```python
import torch

def _softmax(x, axis=-1):
    return torch.softmax(x, dim=axis)

def _gelu(x):
    return torch.nn.functional.gelu(x, approximate='tanh')

def _silu(x):
    return torch.nn.functional.silu(x)

def _sigmoid(x):
    return torch.sigmoid(x)

def _layernorm(x, axis=-1, eps=1e-05):
    m = x.mean(dim=axis, keepdim=True)
    v = x.var(dim=axis, keepdim=True, unbiased=False)
    return (x - m) / torch.sqrt(v + eps)

def _scatter_add(tgt, idx, src, axis=0):
    return tgt.clone().index_add_(axis, idx, src)

def _segment_sum(src, seg, n):
    out = torch.zeros((n,) + src.shape[1:], dtype=src.dtype)
    return out.index_add_(0, seg, src)

def _pad(x, pw):
    flat = tuple((v for pair in reversed(pw) for v in pair))
    return torch.nn.functional.pad(x, flat)
kernel_name = 'adaLN_cond__grouped_kv_mqa__glu_gated_reglu__prompt'

def setup_inputs():

    _w = lambda a: (lambda t: t.to(torch.bfloat16) if t.is_floating_point() else t)(torch.as_tensor(a))

    def _A_setup():
        B, S, D, H = (1, 2048, 512, 32)
        Dh = D // H
        return {'x': _w((lambda *s: torch.randn(*s, dtype=torch.bfloat16) * 0.05)(B, S, D)), 'wq': _w((lambda *s: torch.randn(*s, dtype=torch.bfloat16) * 0.1)(D, H * Dh)), 'wk': _w((lambda *s: torch.randn(*s, dtype=torch.bfloat16) * 0.1)(D, Dh)), 'wv': _w((lambda *s: torch.randn(*s, dtype=torch.bfloat16) * 0.1)(D, Dh)), 'wo': _w((lambda *s: torch.randn(*s, dtype=torch.bfloat16) * 0.1)(H * Dh, D))}
    _A_in = _A_setup()

    def _B_setup():
        B, S, D, F = (1, 2048, 512, 1792)
        return {'x': _w((lambda *s: torch.randn(*s, dtype=torch.bfloat16) * 0.05)(B, S, D)), 'W_gate': _w((lambda *s: torch.randn(*s, dtype=torch.bfloat16) * 1.6)(D, F) * (2.0 / D) ** 0.5), 'W_up': _w((lambda *s: torch.randn(*s, dtype=torch.bfloat16) * 1.6)(D, F) * (2.0 / D) ** 0.5), 'W_down': _w((lambda *s: torch.randn(*s, dtype=torch.bfloat16) * 1.6)(F, D) * (1.0 / F) ** 0.5)}
    _B_in = _B_setup()
    return {'A_x': _A_in['x'], 'A_wq': _A_in['wq'], 'A_wk': _A_in['wk'], 'A_wv': _A_in['wv'], 'A_wo': _A_in['wo'], 'B_x': _B_in['x'], 'B_W_gate': _B_in['W_gate'], 'B_W_up': _B_in['W_up'], 'B_W_down': _B_in['W_down'], 'c': _w((lambda *s: torch.randn(*s, dtype=torch.bfloat16) * 0.2)(1, 512) * 8.0), 'w_mod': _w((lambda *s: torch.randn(*s, dtype=torch.bfloat16) * 0.2)(512, 6 * 512) * 512 ** (-0.5) * 8.0)}

def reference(A_x, A_wq, A_wk, A_wv, A_wo, B_x, B_W_gate, B_W_up, B_W_down, c, w_mod):

    def _A_ref(x, wq, wk, wv, wo):
        B, S, D = x.shape
        H = 32
        Dh = D // H
        q = (lambda x, *d: x.permute(*d))((x @ wq).reshape(B, S, H, Dh), 0, 2, 1, 3)
        k = x @ wk
        v = x @ wv
        scores = torch.einsum('bhqd,bkd->bhqk', q, k) * Dh ** (-0.5)
        p = _softmax(scores, axis=-1)
        o = torch.einsum('bhqk,bkd->bhqd', p, v)
        return (lambda x, *d: x.permute(*d))(o, 0, 2, 1, 3).reshape(B, S, H * Dh) @ wo

    def _B_ref(x, W_gate, W_up, W_down):
        gate = torch.einsum('bsd,df->bsf', x, W_gate)
        up = torch.einsum('bsd,df->bsf', x, W_up)
        gate = gate * (lambda x, dt: x.to(dt))(gate > 0, gate.dtype)
        h = gate * up
        return torch.einsum('bsf,fd->bsd', h, W_down)
    x = A_x
    mod = (_silu(c) @ w_mod).reshape(c.shape[0], 6, 1, -1)
    sa, ca, ga, sb, cb, gb = (mod[:, 0].clone(), mod[:, 1].clone(), mod[:, 2].clone(), mod[:, 3].clone(), mod[:, 4].clone(), mod[:, 5].clone())
    h = _layernorm(x, axis=-1) * (1 + ca) + sa
    x = x + ga * _A_ref(h, A_wq, A_wk, A_wv, A_wo)
    h = _layernorm(x, axis=-1) * (1 + cb) + sb
    x = x + gb * _B_ref(h, B_W_gate, B_W_up, B_W_down)
    return x
```

```cpp
#include <hip/hip_runtime.h>
#include <hip/hip_bf16.h>
#include <stdint.h>

#define DEV __device__ __forceinline__
typedef __bf16 bf16x8 __attribute__((ext_vector_type(8)));
typedef __bf16 bf16x2 __attribute__((ext_vector_type(2)));
typedef float f32x16 __attribute__((ext_vector_type(16)));
typedef float f32x4 __attribute__((ext_vector_type(4)));
typedef float f32x2 __attribute__((ext_vector_type(2)));
typedef unsigned u32x4 __attribute__((ext_vector_type(4)));
typedef unsigned u32x2 __attribute__((ext_vector_type(2)));
typedef unsigned short u16;

#define WS_MODP 0x0u
#define WS_MODF 0xC000u
#define WS_STAT 0x10000u
#define WS_WKVF 0x30000u
#define WS_KF 0x40000u
#define WS_VF 0x50000u
#define WS_WQF 0x80000u
#define WS_WOF 0x100000u
#define WS_WGUF 0x200000u
#define WS_WDF 0x600000u
#define WS_QF 0x800000u
#define WS_OF 0xA00000u
#define WS_X1 0xC00000u
#define WS_YF 0xE00000u
#define WS_HHF 0x1000000u

DEV unsigned pack2(float a, float b) {
  f32x2 v = {a, b};
  bf16x2 r = __builtin_convertvector(v, bf16x2);
  return __builtin_bit_cast(unsigned, r);
}
DEV float bflo(unsigned u) { return __uint_as_float(u << 16); }
DEV float bfhi(unsigned u) { return __uint_as_float(u & 0xffff0000u); }
DEV f32x16 mfma(u32x4 a, u32x4 b, f32x16 c) {
  return __builtin_amdgcn_mfma_f32_32x32x16_bf16(__builtin_bit_cast(bf16x8, a), __builtin_bit_cast(bf16x8, b), c, 0, 0, 0);
}
template <int CTRL> DEV float dppf(float v) {
  return __builtin_bit_cast(float, __builtin_amdgcn_update_dpp(0, __builtin_bit_cast(int, v), CTRL, 0xf, 0xf, true));
}
DEV float red4(float v) { v += dppf<0xB1>(v); v += dppf<0x4E>(v); return v; }
DEV float red8(float v) { v = red4(v); v += dppf<0x141>(v); return v; }
DEV float red16(float v) { v = red8(v); v += dppf<0x140>(v); return v; }
DEV float rl(float v, int l) { return __builtin_bit_cast(float, __builtin_amdgcn_readlane(__builtin_bit_cast(int, v), l)); }
DEV float wave_sum(float v) { v = red16(v); return (rl(v, 0) + rl(v, 16)) + (rl(v, 32) + rl(v, 48)); }
#define GLD(dst, voff, sptr, imm) \
  asm volatile("global_load_dwordx4 %0, %1, %2 offset:%3" : "=v"(dst) : "v"(voff), "s"(sptr), "n"(imm) : "memory")
#define GLD1(dst, voff, sptr, imm) \
  asm volatile("global_load_dword %0, %1, %2 offset:%3" : "=v"(dst) : "v"(voff), "s"(sptr), "n"(imm) : "memory")
DEV void dma16(unsigned voff, const char* sptr, unsigned ldsoff) {
  asm volatile("s_mov_b32 m0, %2\n\ts_nop 0\n\tglobal_load_lds_dwordx4 %0, %1" ::"v"(voff), "s"(sptr), "s"(ldsoff) : "memory");
}
DEV f32x16 zero16() { f32x16 z; for (int i = 0; i < 16; ++i) z[i] = 0.f; return z; }

__global__ __launch_bounds__(256) void k1_prep(const u16* __restrict__ wq, const u16* __restrict__ wk, const u16* __restrict__ wv,
                                               const u16* __restrict__ wo, const u16* __restrict__ wg, const u16* __restrict__ wu,
                                               const u16* __restrict__ wd, const u16* __restrict__ cc, const u16* __restrict__ wmod,
                                               char* __restrict__ ws) {
  __shared__ __attribute__((aligned(16))) u16 tile[64 * 136];
  const int tid = threadIdx.x, lane = tid & 63, w = tid >> 6;
  int b = blockIdx.x;
  if (b < 192) {
    const int cbk = b % 48, kq = b / 48;
    const int c8 = tid & 7, kg = tid >> 3;
    const int k0 = kq * 128 + kg * 4;
    u32x4 wr[4];
#pragma unroll
    for (int i = 0; i < 4; ++i) wr[i] = *(const u32x4*)(wmod + (size_t)(k0 + i) * 3072 + cbk * 64 + c8 * 8);
    u32x2 cr = *(const u32x2*)(cc + k0);
    float cv[4] = {bflo(cr[0]), bfhi(cr[0]), bflo(cr[1]), bfhi(cr[1])};
    float acc[8];
#pragma unroll
    for (int j = 0; j < 8; ++j) acc[j] = 0.f;
#pragma unroll
    for (int i = 0; i < 4; ++i) {
      float s = cv[i] / (1.f + __expf(-cv[i]));
#pragma unroll
      for (int j = 0; j < 4; ++j) { acc[2 * j] += s * bflo(wr[i][j]); acc[2 * j + 1] += s * bfhi(wr[i][j]); }
    }
    float* part = (float*)tile;
#pragma unroll
    for (int j = 0; j < 8; ++j) acc[j] += dppf<0x128>(acc[j]);
    if ((lane & 15) < 8) {
      float* p = part + (w * 4 + (lane >> 4)) * 64 + c8 * 8;
      *(f32x4*)p = f32x4{acc[0], acc[1], acc[2], acc[3]};
      *(f32x4*)(p + 4) = f32x4{acc[4], acc[5], acc[6], acc[7]};
    }
    __syncthreads();
    {
      const int col = tid >> 2, seg = tid & 3;
      float s = 0.f;
#pragma unroll
      for (int i = 0; i < 4; ++i) s += part[(seg * 4 + i) * 64 + col];
      s = red4(s);
      if (seg == 0) ((float*)(ws + WS_MODP))[kq * 3072 + cbk * 64 + col] = s;
    }
    return;
  }
  b -= 192;
  if (b < 8) {
    const int ks = b * 4 + w, h = lane >> 5, m = lane & 31;
    const u16* src = (m < 16 ? wk : wv) + (m & 15) + (ks * 16 + h * 8) * 16;
    u32x4 o;
#pragma unroll
    for (int j = 0; j < 4; ++j) o[j] = (unsigned)src[(2 * j) * 16] | ((unsigned)src[(2 * j + 1) * 16] << 16);
    *(u32x4*)(ws + WS_WKVF + (ks * 64 + lane) * 16) = o;
    return;
  }
  b -= 8;
  const u16 *sl, *sr;
  int ld, perm, gu = 0, KS, t0, ks0;
  char* dst;
  if (b < 64) {
    const int o = b >> 5, bb = b & 31, kb = bb >> 2, nb = bb & 3;
    sl = (o ? wo : wq) + kb * 64 * 512 + nb * 128; sr = sl + 64; ld = 512; perm = o; KS = 32; t0 = nb * 4; ks0 = kb * 4;
    dst = ws + (o ? WS_WOF : WS_WQF);
  } else if (b < 64 + 224) {
    const int bb = b - 64, kb = bb / 28, fb = bb % 28;
    sl = wg + kb * 64 * 1792 + fb * 64; sr = wu + kb * 64 * 1792 + fb * 64; ld = 1792; perm = 0; gu = 1; KS = 33; t0 = fb * 4; ks0 = kb * 4;
    dst = ws + WS_WGUF;
  } else {
    const int bb = b - 288, kb = bb >> 2, nb = bb & 3;
    sl = wd + kb * 64 * 512 + nb * 128; sr = sl + 64; ld = 512; perm = 1; KS = 112; t0 = nb * 4; ks0 = kb * 4;
    dst = ws + WS_WDF;
  }
  u32x4 r[4];
#pragma unroll
  for (int i = 0; i < 4; ++i) {
    const int p = tid + 256 * i, row = p >> 4, c = p & 15;
    r[i] = *(const u32x4*)((c < 8 ? sl : sr) + (size_t)row * ld + (c & 7) * 8);
  }
#pragma unroll
  for (int i = 0; i < 4; ++i) {
    const int p = tid + 256 * i, row = p >> 4, c = p & 15;
    *(u32x4*)(tile + row * 136 + c * 8) = r[i];
  }
  __syncthreads();
  const int h = lane >> 5, m = lane & 31;
  const int ka = w * 16 + (perm ? 4 * h : 8 * h), kb2 = ka + (perm ? 8 : 4);
#pragma unroll
  for (int tt = 0; tt < 4; ++tt) {
    const int n = gu ? ((m < 16 ? 0 : 64) + tt * 16 + (m & 15)) : (tt * 32 + m);
    const u16* pa = tile + ka * 136 + n;
    const u16* pb = tile + kb2 * 136 + n;
    u32x4 o;
    o[0] = (unsigned)pa[0] | ((unsigned)pa[136] << 16);
    o[1] = (unsigned)pa[272] | ((unsigned)pa[408] << 16);
    o[2] = (unsigned)pb[0] | ((unsigned)pb[136] << 16);
    o[3] = (unsigned)pb[272] | ((unsigned)pb[408] << 16);
    *(u32x4*)(dst + ((size_t)((t0 + tt) * KS + ks0 + w) * 64 + lane) * 16) = o;
  }
}

DEV void split_bf(float v, unsigned& hi, unsigned& lo) {
  unsigned p = pack2(v, 0.f) & 0xffffu;
  float r = v - __uint_as_float(p << 16);
  hi = p; lo = pack2(r, 0.f) & 0xffffu;
}

template <bool iskv> DEV void qkv_body(const u16* __restrict__ x, char* __restrict__ ws, char* smem, const int rt, const int cb) {
  const int tid = threadIdx.x, lane = tid & 63;
  const int w = __builtin_amdgcn_readfirstlane(tid >> 6);
  const float* modp = (const float*)(ws + WS_MODP);
  float* sc = (float*)(smem + 33792 + 32768);
  const unsigned l16 = lane * 16;
  u32x4 xr[4];
  {
    const char* xp = (const char*)(x + (size_t)(rt * 32 + w * 4) * 512);
    GLD(xr[0], l16, xp, 0); GLD(xr[1], l16, xp, 1024); GLD(xr[2], l16, xp, 2048); GLD(xr[3], l16, xp, 3072);
  }
  float mp[8];
#pragma unroll
  for (int i = 0; i < 4; ++i) { GLD1(mp[i], tid * 4, modp + i * 3072, 0); GLD1(mp[4 + i], tid * 4, modp + i * 3072, 2048); }
  u32x4 wf[16];
  {
    const char* p = iskv ? ws + WS_WKVF + w * 4096 : ws + WS_WQF + (size_t)((cb * 4 + (w & 3)) * 32 + (w >> 2) * 16) * 1024;
    GLD(wf[0], l16, p, 0); GLD(wf[1], l16, p, 1024); GLD(wf[2], l16, p, 2048); GLD(wf[3], l16, p, 3072);
    if (!iskv) {
#pragma unroll
      for (int i = 1; i < 4; ++i) {
        const char* q = p + i * 4096;
        GLD(wf[4 * i], l16, q, 0); GLD(wf[4 * i + 1], l16, q, 1024); GLD(wf[4 * i + 2], l16, q, 2048); GLD(wf[4 * i + 3], l16, q, 3072);
      }
      asm volatile("s_waitcnt vmcnt(16)" : "+v"(xr[0]), "+v"(xr[1]), "+v"(xr[2]), "+v"(xr[3]), "+v"(mp[0]), "+v"(mp[1]), "+v"(mp[2]),
                   "+v"(mp[3]), "+v"(mp[4]), "+v"(mp[5]), "+v"(mp[6]), "+v"(mp[7]));
    } else {
      asm volatile("s_waitcnt vmcnt(4)" : "+v"(xr[0]), "+v"(xr[1]), "+v"(xr[2]), "+v"(xr[3]), "+v"(mp[0]), "+v"(mp[1]), "+v"(mp[2]),
                   "+v"(mp[3]), "+v"(mp[4]), "+v"(mp[5]), "+v"(mp[6]), "+v"(mp[7]));
    }
  }
  sc[tid] = (mp[0] + mp[1]) + (mp[2] + mp[3]);
  sc[512 + tid] = 1.f + ((mp[4] + mp[5]) + (mp[6] + mp[7]));
  float v[4][8];
#pragma unroll
  for (int i = 0; i < 4; ++i) {
#pragma unroll
    for (int j = 0; j < 4; ++j) { v[i][2 * j] = bflo(xr[i][j]); v[i][2 * j + 1] = bfhi(xr[i][j]); }
    float s = ((v[i][0] + v[i][1]) + (v[i][2] + v[i][3])) + ((v[i][4] + v[i][5]) + (v[i][6] + v[i][7]));
    const float mean = wave_sum(s) * (1.f / 512.f);
    float q = 0.f;
#pragma unroll
    for (int j = 0; j < 8; ++j) { v[i][j] -= mean; q += v[i][j] * v[i][j]; }
    const float r = 1.f / sqrtf(wave_sum(q) * (1.f / 512.f) + 1e-5f);
#pragma unroll
    for (int j = 0; j < 8; ++j) v[i][j] *= r;
  }
  asm volatile("s_waitcnt lgkmcnt(0)\n\ts_barrier" ::: "memory");
  {
    const f32x4 s0 = *(const f32x4*)(sc + lane * 8), s1 = *(const f32x4*)(sc + lane * 8 + 4);
    const f32x4 c0 = *(const f32x4*)(sc + 512 + lane * 8), c1 = *(const f32x4*)(sc + 512 + lane * 8 + 4);
#pragma unroll
    for (int i = 0; i < 4; ++i) {
      u32x4 o;
      o[0] = pack2(v[i][0] * c0[0] + s0[0], v[i][1] * c0[1] + s0[1]);
      o[1] = pack2(v[i][2] * c0[2] + s0[2], v[i][3] * c0[3] + s0[3]);
      o[2] = pack2(v[i][4] * c1[0] + s1[0], v[i][5] * c1[1] + s1[1]);
      o[3] = pack2(v[i][6] * c1[2] + s1[2], v[i][7] * c1[3] + s1[3]);
      *(u32x4*)(smem + lane * 528 + (w * 4 + i) * 16) = o;
    }
  }
  asm volatile("s_waitcnt vmcnt(0)" : "+v"(wf[0]), "+v"(wf[1]), "+v"(wf[2]), "+v"(wf[3]));
  if (!iskv)
    asm volatile("" : "+v"(wf[4]), "+v"(wf[5]), "+v"(wf[6]), "+v"(wf[7]), "+v"(wf[8]), "+v"(wf[9]), "+v"(wf[10]), "+v"(wf[11]),
                 "+v"(wf[12]), "+v"(wf[13]), "+v"(wf[14]), "+v"(wf[15]));
  if (iskv && rt < 6) {
    const int i = rt * 512 + tid;
    ((float*)(ws + WS_MODF))[i] = (modp[i] + modp[3072 + i]) + (modp[6144 + i] + modp[9216 + i]);
  }
  __syncthreads();
  const char* hb = smem + (lane >> 5) * 528 + (lane & 31) * 16;
  char* red = smem + 33792;
  f32x16 acc = zero16();
  if (iskv) {
#pragma unroll
    for (int i = 0; i < 4; ++i) acc = mfma(wf[i], *(const u32x4*)(hb + (w * 4 + i) * 1056), acc);
#pragma unroll
    for (int q = 0; q < 4; ++q)
      *(f32x4*)(red + (w * 4 + q) * 1024 + lane * 16) = f32x4{acc[4 * q], acc[4 * q + 1], acc[4 * q + 2], acc[4 * q + 3]};
    __syncthreads();
    if (tid < 256) {
      const int q = w;
      f32x4 s = {0, 0, 0, 0};
#pragma unroll
      for (int p = 0; p < 8; ++p) s += *(const f32x4*)(red + (p * 4 + q) * 1024 + lane * 16);
      const unsigned p0 = pack2(s[0], s[1]), p1 = pack2(s[2], s[3]);
      if (q < 2) {
        *(u32x2*)(ws + WS_KF + rt * 1024 + lane * 16 + q * 8) = u32x2{p0, p1};
      } else {
        const int t = lane & 31, h = lane >> 5;
        const int d0 = 8 * (q - 2) + 4 * h;
        u16* vp = (u16*)(ws + WS_VF + rt * 1024 + (t >> 4) * 512 + ((t >> 2) & 1) * 256 + d0 * 16) + (((t >> 3) & 1) * 4 + (t & 3));
        vp[0] = (u16)p0; vp[8] = (u16)(p0 >> 16); vp[16] = (u16)p1; vp[24] = (u16)(p1 >> 16);
      }
    }
  } else {
    const int kh = w >> 2, ct = w & 3;
#pragma unroll
    for (int i = 0; i < 16; ++i) acc = mfma(wf[i], *(const u32x4*)(hb + (kh * 16 + i) * 1056), acc);
    f32x4 k0, k1, g0, g1;
    if (kh == 0) {
      k0 = f32x4{acc[0], acc[1], acc[2], acc[3]}; k1 = f32x4{acc[4], acc[5], acc[6], acc[7]};
      g0 = f32x4{acc[8], acc[9], acc[10], acc[11]}; g1 = f32x4{acc[12], acc[13], acc[14], acc[15]};
    } else {
      g0 = f32x4{acc[0], acc[1], acc[2], acc[3]}; g1 = f32x4{acc[4], acc[5], acc[6], acc[7]};
      k0 = f32x4{acc[8], acc[9], acc[10], acc[11]}; k1 = f32x4{acc[12], acc[13], acc[14], acc[15]};
    }
    *(f32x4*)(red + w * 2048 + lane * 16) = g0;
    *(f32x4*)(red + w * 2048 + 1024 + lane * 16) = g1;
    __syncthreads();
    k0 += *(const f32x4*)(red + (w ^ 4) * 2048 + lane * 16);
    k1 += *(const f32x4*)(red + (w ^ 4) * 2048 + 1024 + lane * 16);
    const float sc = 0.25f * 1.4426950408889634f;
    k0 *= sc; k1 *= sc;
    u32x4 o = {pack2(k0[0], k0[1]), pack2(k0[2], k0[3]), pack2(k1[0], k1[1]), pack2(k1[2], k1[3])};
    const int head = (cb * 4 + ct) * 2 + kh;
    *(u32x4*)(ws + WS_QF + ((size_t)(rt * 32 + head) * 64 + lane) * 16) = o;
  }
}

__global__ __launch_bounds__(512, 4) void k2_qkv(const u16* __restrict__ x, const u16* __restrict__ wg, const u16* __restrict__ wu,
                                                 char* __restrict__ ws) {
  __shared__ __attribute__((aligned(16))) char smem[33792 + 32768 + 4096];
  const int tid = threadIdx.x, lane = tid & 63;
  const int w = __builtin_amdgcn_readfirstlane(tid >> 6);
  const float* modp = (const float*)(ws + WS_MODP);
  float* sc = (float*)(smem + 33792 + 32768);
  int b = blockIdx.x;
  if (b < 56) {
    const int up = b >= 28, f0 = (b % 28) * 64;
    const int c8 = tid & 7, kg = tid >> 3;
    const u16* src = (up ? wu : wg) + (size_t)(kg * 8) * 1792 + f0 + c8 * 8;
    u32x4 wr[8];
#pragma unroll
    for (int i = 0; i < 8; ++i) wr[i] = *(const u32x4*)(src + i * 1792);
    {
      const float* mp = modp + 1536 + tid;
      sc[tid] = (mp[0] + mp[3072]) + (mp[6144] + mp[9216]);
      sc[512 + tid] = 1.f + ((mp[512] + mp[3584]) + (mp[6656] + mp[9728]));
    }
    __syncthreads();
    const f32x4 s0 = *(const f32x4*)(sc + kg * 8), s1 = *(const f32x4*)(sc + kg * 8 + 4);
    const f32x4 c0 = *(const f32x4*)(sc + 512 + kg * 8), c1 = *(const f32x4*)(sc + 512 + kg * 8 + 4);
    float au[8], ab[8];
#pragma unroll
    for (int j = 0; j < 8; ++j) au[j] = ab[j] = 0.f;
#pragma unroll
    for (int i = 0; i < 8; ++i) {
      const float c = i < 4 ? c0[i & 3] : c1[i & 3], s = i < 4 ? s0[i & 3] : s1[i & 3];
#pragma unroll
      for (int j = 0; j < 4; ++j) {
        const float lo = bflo(wr[i][j]), hi = bfhi(wr[i][j]);
        au[2 * j] += c * lo; au[2 * j + 1] += c * hi; ab[2 * j] += s * lo; ab[2 * j + 1] += s * hi;
      }
    }
#pragma unroll
    for (int j = 0; j < 8; ++j) { au[j] += dppf<0x128>(au[j]); ab[j] += dppf<0x128>(ab[j]); }
    float* part = (float*)smem;
    if ((lane & 15) < 8) {
      float* p = part + (w * 4 + (lane >> 4)) * 128 + c8 * 16;
      *(f32x4*)p = f32x4{au[0], au[1], au[2], au[3]};
      *(f32x4*)(p + 4) = f32x4{au[4], au[5], au[6], au[7]};
      *(f32x4*)(p + 8) = f32x4{ab[0], ab[1], ab[2], ab[3]};
      *(f32x4*)(p + 12) = f32x4{ab[4], ab[5], ab[6], ab[7]};
    }
    __syncthreads();
    const int col = tid >> 3, seg = tid & 7;
    float u = 0.f, bb = 0.f;
#pragma unroll
    for (int i = 0; i < 4; ++i) {
      const float* p = part + (seg * 4 + i) * 128 + (col >> 3) * 16 + (col & 7);
      u += p[0]; bb += p[8];
    }
    u = red8(u); bb = red8(bb);
    if (seg < 2) {
      unsigned uh, ul, bh, bl;
      split_bf(u, uh, ul); split_bf(bb, bh, bl);
      u32x4 o = {uh | (ul << 16), uh | (ul << 16), bh | (bl << 16), bh | (bl << 16)};
      if (seg) o = u32x4{0, 0, 0, 0};
      const int f = f0 + col, t = f >> 4, m = (f & 15) + 16 * up + 32 * seg;
      *(u32x4*)(ws + WS_WGUF + ((size_t)(t * 33 + 32) * 64 + m) * 16) = o;
    }
    return;
  }
  b -= 56;
  if (b < 64) qkv_body<true>(x, ws, smem, b, 0);
  else qkv_body<false>(x, ws, smem, b & 63, (b >> 6) - 1);
}

DEV float xhalf_max(float m) { return fmaxf(m, __shfl_xor(m, 32)); }
DEV float max16(const f32x16& s) {
  float a = fmaxf(fmaxf(s[0], s[1]), s[2]), b = fmaxf(fmaxf(s[3], s[4]), s[5]), c = fmaxf(fmaxf(s[6], s[7]), s[8]);
  float d = fmaxf(fmaxf(s[9], s[10]), s[11]), e = fmaxf(fmaxf(s[12], s[13]), s[14]);
  return fmaxf(fmaxf(fmaxf(a, b), c), fmaxf(fmaxf(d, e), s[15]));
}
DEV void softpv(const f32x16& s, const u32x4& v0, const u32x4& v1, f32x16& o) {
  u32x4 p0, p1;
#pragma unroll
  for (int i = 0; i < 4; ++i) p0[i] = pack2(__builtin_amdgcn_exp2f(s[2 * i]), __builtin_amdgcn_exp2f(s[2 * i + 1]));
  o = mfma(v0, p0, o);
#pragma unroll
  for (int i = 0; i < 4; ++i) p1[i] = pack2(__builtin_amdgcn_exp2f(s[8 + 2 * i]), __builtin_amdgcn_exp2f(s[9 + 2 * i]));
  o = mfma(v1, p1, o);
}
#define LDK(dst, addr, off) asm volatile("ds_read_b128 %0, %1 offset:" #off : "=v"(dst) : "v"(addr))
#define LDV(d0, d1, addr, off0, off1)                                                                                   \
  asm volatile("s_mov_b64 exec, %3\n\tds_read_b128 %0, %2 offset:" #off0 "\n\tds_read_b128 %1, %2 offset:" #off1        \
               "\n\ts_mov_b64 exec, -1"                                                                                 \
               : "+v"(d0), "+v"(d1)                                                                                     \
               : "v"(addr), "s"(vmask))
#define LWAIT(a, b, c) asm volatile("s_waitcnt lgkmcnt(0)" : "+v"(a), "+v"(b), "+v"(c))

__global__ __launch_bounds__(512, 2) void k3_attn(char* __restrict__ ws) {
  __shared__ __attribute__((aligned(16))) char smem[131072 + 4096];
  const int tid = threadIdx.x, lane = tid & 63;
  const int w = __builtin_amdgcn_readfirstlane(tid >> 6);
  const int rt = blockIdx.x & 63, hg = blockIdx.x >> 6;
  const int pu = w & 3, kh = w >> 2;
  const unsigned sbase = (unsigned)(uintptr_t)smem;
  const int hA = hg * 8 + pu * 2;
  u32x4 qA, qB;
  {
    const unsigned l16 = lane * 16;
    const char* qp = ws + WS_QF + (size_t)(rt * 32 + hA) * 1024;
    GLD(qA, l16, qp, 0); GLD(qB, l16, qp, 1024);
    const char* kp = ws + WS_KF + w * 1024;
    const unsigned lb = sbase + w * 1024;
#pragma unroll
    for (int i = 0; i < 4; ++i) {
      dma16(l16, kp + i * 8192, lb + i * 8192);
      dma16(l16, kp + i * 8192 + 32768, lb + i * 8192 + 32768);
      dma16(l16, kp + i * 8192 + 65536, lb + i * 8192 + 65536);
      dma16(l16, kp + i * 8192 + 32768 + 65536, lb + i * 8192 + 32768 + 65536);
    }
  }
  const unsigned long long vmask = 0x0000ffff0000ffffull;
  const unsigned kbase = sbase + kh * 32768 + lane * 16;
  const unsigned vbase = sbase + 65536 + kh * 32768 + (lane >> 5) * 256 + (lane & 15) * 16;
  const u32x4 ones = {0x3f803f80u, 0x3f803f80u, 0x3f803f80u, 0x3f803f80u};
  u32x4 va0 = ones, va1 = ones, vb0 = ones, vb1 = ones, ka, kb;
  asm volatile("s_waitcnt vmcnt(12)\n\ts_barrier" : "+v"(qA), "+v"(qB)::"memory");
  f32x16 oA, oB;
  float offA, offB;
  for (int attempt = 0; attempt < 2; ++attempt) {
    float mA = -3.0e38f, mB = -3.0e38f;
    const int ntm = attempt ? 32 : 1;
    for (int t = 0; t < ntm; ++t) {
      const unsigned a = kbase + t * 1024;
      LDK(ka, a, 0);
      LWAIT(ka, va0, va1);
      f32x16 sA = mfma(ka, qA, zero16()), sB = mfma(ka, qB, zero16());
      mA = fmaxf(mA, max16(sA)); mB = fmaxf(mB, max16(sB));
    }
    mA = xhalf_max(mA); mB = xhalf_max(mB);
    const float margin = attempt ? 0.f : 40.f;
    const float thr = attempt ? __builtin_inff() : 5.9604645e-8f;
    offA = mA + margin; offB = mB + margin;
    f32x16 cA, cB;
#pragma unroll
    for (int i = 0; i < 16; ++i) { cA[i] = -offA; cB[i] = -offB; }
    oA = zero16(); oB = zero16();
    unsigned kad = kbase, vad = vbase;
    LDK(ka, kad, 0);
    LDK(kb, kad, 1024);
    LDV(va0, va1, vad, 0, 512);
    asm volatile("s_waitcnt lgkmcnt(0)" : "+v"(ka), "+v"(kb), "+v"(va0), "+v"(va1));
    f32x16 sA0 = mfma(ka, qA, cA), sB0 = mfma(ka, qB, cB), sA1, sB1;
    for (int p = 0; p < 16; ++p) {
      if (p == 2 && attempt == 0) asm volatile("s_waitcnt vmcnt(0)\n\ts_barrier" ::: "memory");
      if (p == 1 || p == 2 || p == 4 || p == 8) {
        if (__any(oA[8] > thr || oB[8] > thr)) {
          const int eA = __builtin_amdgcn_frexp_expf(oA[8]) + 40, eB = __builtin_amdgcn_frexp_expf(oB[8]) + 40;
          const float fA = (float)eA, fB = (float)eB;
#pragma unroll
          for (int i = 0; i < 9; ++i) { oA[i] = __builtin_amdgcn_ldexpf(oA[i], -eA); oB[i] = __builtin_amdgcn_ldexpf(oB[i], -eB); }
          offA += fA; offB += fB;
#pragma unroll
          for (int i = 0; i < 16; ++i) { cA[i] = -offA; cB[i] = -offB; sA0[i] -= fA; sB0[i] -= fB; }
        }
      }
      LDK(ka, kad, 2048);
      LDV(vb0, vb1, vad, 1024, 1536);
      sA1 = mfma(kb, qA, cA);
      softpv(sA0, va0, va1, oA);
      sB1 = mfma(kb, qB, cB);
      softpv(sB0, va0, va1, oB);
      LWAIT(ka, vb0, vb1);
      LDK(kb, kad, 3072);
      LDV(va0, va1, vad, 2048, 2560);
      sA0 = mfma(ka, qA, cA);
      softpv(sA1, vb0, vb1, oA);
      sB0 = mfma(ka, qB, cB);
      softpv(sB1, vb0, vb1, oB);
      LWAIT(kb, va0, va1);
      kad += 2048; vad += 2048;
    }
    if (attempt) break;
    float aA = 0.f, aB = 0.f;
#pragma unroll
    for (int i = 0; i < 8; ++i) { aA = fmaxf(aA, fabsf(oA[i])); aB = fmaxf(aB, fabsf(oB[i])); }
    float sA = oA[8], sB = oB[8];
#pragma unroll
    for (int i = 0; i < 8; ++i) { sA += oA[i] * 0.f; sB += oB[i] * 0.f; }
    const float tiny = 7.8886091e-31f;
    const bool ok = (sA < 3.0e38f) && (sB < 3.0e38f) && (aA >= tiny) && (aB >= tiny);
    if (!__any(!ok)) break;
  }
  __syncthreads();
  f32x4 k0, k1, g0, g1;
  f32x2 k2, g2;
  {
    const f32x4 a0 = {oA[0], oA[1], oA[2], oA[3]}, a1 = {oA[4], oA[5], oA[6], oA[7]};
    const f32x4 b0 = {oB[0], oB[1], oB[2], oB[3]}, b1 = {oB[4], oB[5], oB[6], oB[7]};
    const f32x2 a2 = {oA[8], offA}, b2 = {oB[8], offB};
    if (kh == 0) { k0 = a0; k1 = a1; k2 = a2; g0 = b0; g1 = b1; g2 = b2; }
    else { k0 = b0; k1 = b1; k2 = b2; g0 = a0; g1 = a1; g2 = a2; }
  }
  *(f32x4*)(smem + w * 3072 + lane * 16) = g0;
  *(f32x4*)(smem + w * 3072 + 1024 + lane * 16) = g1;
  *(f32x2*)(smem + w * 3072 + 2048 + lane * 8) = g2;
  __syncthreads();
  g0 = *(const f32x4*)(smem + (w ^ 4) * 3072 + lane * 16);
  g1 = *(const f32x4*)(smem + (w ^ 4) * 3072 + 1024 + lane * 16);
  g2 = *(const f32x2*)(smem + (w ^ 4) * 3072 + 2048 + lane * 8);
  const float m = fmaxf(k2[1], g2[1]);
  float fk = __builtin_amdgcn_exp2f(k2[1] - m), fg = __builtin_amdgcn_exp2f(g2[1] - m);
  const float inv = 1.f / (k2[0] * fk + g2[0] * fg);
  fk *= inv; fg *= inv;
  k0 = k0 * fk + g0 * fg; k1 = k1 * fk + g1 * fg;
  u32x4 o = {pack2(k0[0], k0[1]), pack2(k0[2], k0[3]), pack2(k1[0], k1[1]), pack2(k1[2], k1[3])};
  *(u32x4*)(ws + WS_OF + ((size_t)(rt * 32 + hA + kh) * 64 + lane) * 16) = o;
}

template <int KS, int DEPTH, bool FINAL>
__global__ __launch_bounds__(256, 1) void k46_gemm(const u16* __restrict__ res, const char* __restrict__ af, const char* __restrict__ wf,
                                                   char* __restrict__ ws, u16* __restrict__ out) {
  __shared__ __attribute__((aligned(16))) float red[4 * 64 * 68];
  const int tid = threadIdx.x, lane = tid & 63;
  const int w = __builtin_amdgcn_readfirstlane(tid >> 6);
  const int b = blockIdx.x;
  const int rb = (b & 7) * 4 + ((b >> 3) & 3), cbk = b >> 5;
  constexpr int N = KS / 4;
  const unsigned l16 = lane * 16;
  const char* ap = af + (size_t)(rb * 2 * KS + w * N) * 1024;
  const char* wp = wf + (size_t)(cbk * 2 * KS + w * N) * 1024;
  u32x4 fa[DEPTH][2], fw[DEPTH][2];
#define LD4(s, k)                                                                                     \
  {                                                                                                   \
    GLD(fa[s][0], l16, ap + ((k) >> 2) * 4096, ((k) & 3) * 1024);                                     \
    GLD(fw[s][0], l16, wp + ((k) >> 2) * 4096, ((k) & 3) * 1024);                                     \
    GLD(fa[s][1], l16, ap + KS * 1024 + ((k) >> 2) * 4096, ((k) & 3) * 1024);                         \
    GLD(fw[s][1], l16, wp + KS * 1024 + ((k) >> 2) * 4096, ((k) & 3) * 1024);                         \
  }
#pragma unroll
  for (int i = 0; i < DEPTH; ++i) LD4(i, i)
  const int cg = tid & 7, r0 = tid >> 3;
  const int col = cbk * 64 + cg * 8;
  const float* modf = (const float*)(ws + WS_MODF);
  u32x4 xr[2], g0u, g1u, c0u, c1u;
  {
    const char* rp = (const char*)(res + (size_t)(rb * 64) * 512 + cbk * 64);
    const unsigned ro = r0 * 1024 + cg * 16;
    GLD(xr[0], ro, rp, 0); GLD(xr[1], ro, rp + 32768, 0);
    const unsigned mo = cg * 32;
    const char* gp = (const char*)(modf + (FINAL ? 2560 : 1024) + cbk * 64);
    GLD(g0u, mo, gp, 0); GLD(g1u, mo, gp, 16);
    const char* cp = (const char*)(modf + 2048 + cbk * 64);
    GLD(c0u, mo, cp, 0); GLD(c1u, mo, cp, 16);
  }
  f32x16 acc[2][2];
  acc[0][0] = acc[0][1] = acc[1][0] = acc[1][1] = zero16();
#pragma unroll
  for (int k = 0; k < N; ++k) {
    const int s = k % DEPTH;
    const int after = 4 * ((k + DEPTH < N ? k + DEPTH : N) - k - 1) + (k < DEPTH ? 6 : 0);
    asm volatile("s_waitcnt vmcnt(%4)" : "+v"(fa[s][0]), "+v"(fa[s][1]), "+v"(fw[s][0]), "+v"(fw[s][1]) : "n"(after));
    acc[0][0] = mfma(fw[s][0], fa[s][0], acc[0][0]);
    acc[0][1] = mfma(fw[s][0], fa[s][1], acc[0][1]);
    acc[1][0] = mfma(fw[s][1], fa[s][0], acc[1][0]);
    acc[1][1] = mfma(fw[s][1], fa[s][1], acc[1][1]);
    if (k + DEPTH < N) LD4(s, k + DEPTH)
  }
  asm volatile("s_waitcnt vmcnt(0)" : "+v"(xr[0]), "+v"(xr[1]), "+v"(g0u), "+v"(g1u), "+v"(c0u), "+v"(c1u));
  const f32x4 g0 = __builtin_bit_cast(f32x4, g0u), g1 = __builtin_bit_cast(f32x4, g1u);
  const f32x4 c0 = __builtin_bit_cast(f32x4, c0u) + 1.f, c1 = __builtin_bit_cast(f32x4, c1u) + 1.f;
  {
    float* rp = red + w * (64 * 68) + (lane & 31) * 68 + (lane >> 5) * 4;
#pragma unroll
    for (int i = 0; i < 2; ++i)
#pragma unroll
      for (int j = 0; j < 2; ++j)
#pragma unroll
        for (int q = 0; q < 4; ++q)
          *(f32x4*)(rp + j * 32 * 68 + i * 32 + q * 8) =
              f32x4{acc[i][j][4 * q], acc[i][j][4 * q + 1], acc[i][j][4 * q + 2], acc[i][j][4 * q + 3]};
  }
  __syncthreads();
#pragma unroll
  for (int e = 0; e < 2; ++e) {
    const int r = r0 + 32 * e, row = rb * 64 + r;
    f32x4 s0 = {0, 0, 0, 0}, s1 = s0;
#pragma unroll
    for (int p = 0; p < 4; ++p) {
      const float* rp = red + p * (64 * 68) + r * 68 + cg * 8;
      s0 += *(const f32x4*)rp; s1 += *(const f32x4*)(rp + 4);
    }
    const f32x4 x0 = {bflo(xr[e][0]), bfhi(xr[e][0]), bflo(xr[e][1]), bfhi(xr[e][1])};
    const f32x4 x1 = {bflo(xr[e][2]), bfhi(xr[e][2]), bflo(xr[e][3]), bfhi(xr[e][3])};
    s0 = x0 + g0 * s0; s1 = x1 + g1 * s1;
    const u32x4 o = {pack2(s0[0], s0[1]), pack2(s0[2], s0[3]), pack2(s1[0], s1[1]), pack2(s1[2], s1[3])};
    *(u32x4*)(out + (size_t)row * 512 + col) = o;
    if (!FINAL) {
      const f32x4 y0 = s0 * c0, y1 = s1 * c1;
      const u32x4 y = {pack2(y0[0], y0[1]), pack2(y0[2], y0[3]), pack2(y1[0], y1[1]), pack2(y1[2], y1[3])};
      *(u32x4*)(ws + WS_YF + (size_t)((row >> 5) * 32 + (col >> 4)) * 1024 + ((col >> 3) & 1) * 512 + (row & 31) * 16) = y;
      const float mean = red8(((s0[0] + s0[1]) + (s0[2] + s0[3])) + ((s1[0] + s1[1]) + (s1[2] + s1[3]))) * (1.f / 64.f);
      s0 -= mean; s1 -= mean;
      s0 *= s0; s1 *= s1;
      const float m2 = red8(((s0[0] + s0[1]) + (s0[2] + s0[3])) + ((s1[0] + s1[1]) + (s1[2] + s1[3])));
      if (cg == 0) *(f32x2*)(ws + WS_STAT + (size_t)(row * 8 + cbk) * 8) = f32x2{mean, m2};
    }
  }
}


__global__ __launch_bounds__(256, 1) void k5_ffn(char* __restrict__ ws) {
  __shared__ __attribute__((aligned(16))) char smem[5 * 28672 + 7168];
  const int tid = threadIdx.x, lane = tid & 63;
  const int w = __builtin_amdgcn_readfirstlane(tid >> 6);
  const int b = blockIdx.x, xc = b & 7, idx = b >> 3;
  const int rb = (xc & 1) * 8 + (idx & 7), cbk = (xc >> 1) * 4 + (idx >> 3);
  const int rt = rb * 4 + w, t0 = cbk * 7;
  const unsigned sbase = (unsigned)(uintptr_t)smem;
  const unsigned l16 = lane * 16;
  const char* wsrc = ws + WS_WGUF + (size_t)t0 * 33 * 1024;
  const char* ysrc = ws + WS_YF + (size_t)rt * 32 * 1024;
  u32x4 st[4], y[32];
  {
    const char* sp = ws + WS_STAT + (size_t)(rt * 32) * 64;
    const unsigned so = (lane & 31) * 64;
    GLD(st[0], so, sp, 0); GLD(st[1], so, sp, 16); GLD(st[2], so, sp, 32); GLD(st[3], so, sp, 48);
    const int i2 = w + 4 < 7 ? w + 4 : 6;
    dma16(l16, wsrc + (w * 33 + 32) * 1024, sbase + 5 * 28672 + w * 1024);
    dma16(l16, wsrc + (i2 * 33 + 32) * 1024, sbase + 5 * 28672 + i2 * 1024);
  }
#define ISSUE(c)                                                                                             \
  {                                                                                                          \
    _Pragma("unroll") for (int i = 0; i < 7; ++i)                                                            \
        dma16(l16, wsrc + (i * 33 + 4 * (c) + w) * 1024, sbase + ((c) % 5) * 28672 + (w * 7 + i) * 1024);    \
    const char* yp = ysrc + (c) * 4096;                                                                      \
    GLD(y[4 * (c)], l16, yp, 0); GLD(y[4 * (c) + 1], l16, yp, 1024);                                         \
    GLD(y[4 * (c) + 2], l16, yp, 2048); GLD(y[4 * (c) + 3], l16, yp, 3072);                                  \
  }
#define WAITB(n, c)                                                                                          \
  asm volatile("s_waitcnt vmcnt(" #n ")\n\ts_barrier"                                                        \
               : "+v"(y[4 * (c)]), "+v"(y[4 * (c) + 1]), "+v"(y[4 * (c) + 2]), "+v"(y[4 * (c) + 3])::"memory")
  ISSUE(0) ISSUE(1) ISSUE(2) ISSUE(3) ISSUE(4)
  f32x16 acc[7];
#pragma unroll
  for (int i = 0; i < 7; ++i) acc[i] = zero16();
  const char* lp = smem + lane * 16;
  u32x4 wc[7], wn[7];
  WAITB(44, 0);
  asm volatile("" : "+v"(st[0]), "+v"(st[1]), "+v"(st[2]), "+v"(st[3]));
#pragma unroll
  for (int i = 0; i < 7; ++i) wc[i] = *(const u32x4*)(lp + i * 1024);
  float r2;
  u32x4 yx;
  {
    float mb[8], m2 = 0.f, mean = 0.f;
#pragma unroll
    for (int i = 0; i < 4; ++i) {
      mb[2 * i] = __uint_as_float(st[i][0]); mb[2 * i + 1] = __uint_as_float(st[i][2]);
      m2 += __uint_as_float(st[i][1]) + __uint_as_float(st[i][3]);
      mean += mb[2 * i] + mb[2 * i + 1];
    }
    mean *= 0.125f;
    float d = 0.f;
#pragma unroll
    for (int i = 0; i < 8; ++i) d += (mb[i] - mean) * (mb[i] - mean);
    const float var = (m2 + 64.f * d) * (1.f / 512.f) + 1e-5f;
    r2 = 1.f / var;
    unsigned mh, ml, rh, rlo;
    split_bf(-mean, mh, ml); split_bf(sqrtf(var), rh, rlo);
    yx = u32x4{mh | (mh << 16), ml | (ml << 16), rh | (rh << 16), rlo | (rlo << 16)};
    if (lane >= 32) yx = u32x4{0, 0, 0, 0};
  }
#define KSTEP(g, CUR, NXT)                                                                                   \
  {                                                                                                          \
    const char* np = lp + ((g) == 31 ? 5 * 28672 : ((((g) + 1) / 4) % 5) * 28672 + (((g) + 1) % 4) * 7168);  \
    _Pragma("unroll") for (int i = 0; i < 7; ++i) NXT[i] = *(const u32x4*)(np + i * 1024);                   \
    _Pragma("unroll") for (int i = 0; i < 7; ++i) acc[i] = mfma(CUR[i], y[g], acc[i]);                       \
  }
#define CHUNK(c, PRE)                                                                                        \
  KSTEP(4 * (c), wc, wn) KSTEP(4 * (c) + 1, wn, wc) KSTEP(4 * (c) + 2, wc, wn) PRE KSTEP(4 * (c) + 3, wn, wc)
  CHUNK(0, WAITB(33, 1);)
  CHUNK(1, WAITB(22, 2); ISSUE(5))
  CHUNK(2, WAITB(22, 3); ISSUE(6))
  CHUNK(3, WAITB(22, 4); ISSUE(7))
  CHUNK(4, WAITB(22, 5);)
  CHUNK(5, WAITB(11, 6);)
  CHUNK(6, WAITB(0, 7);)
  CHUNK(7, )
  char* hp = ws + WS_HHF + ((size_t)(rt * 112 + t0) * 64 + lane) * 16;
#pragma unroll
  for (int i = 0; i < 7; ++i) {
    const f32x16 a = mfma(wc[i], yx, acc[i]);
    float hv[8];
#pragma unroll
    for (int j = 0; j < 8; ++j) hv[j] = fmaxf(a[j], 0.f) * r2 * a[j + 8];
    *(u32x4*)(hp + i * 1024) = u32x4{pack2(hv[0], hv[1]), pack2(hv[2], hv[3]), pack2(hv[4], hv[5]), pack2(hv[6], hv[7])};
  }
}

extern "C" void kernel_launch(void* const* d_in, const int* in_sizes, int n_in, void* d_out, int out_size, void* d_ws,
                              size_t ws_size, hipStream_t stream) {
  const u16* x = (const u16*)d_in[0];
  const u16* wq = (const u16*)d_in[1];
  const u16* wk = (const u16*)d_in[2];
  const u16* wv = (const u16*)d_in[3];
  const u16* wo = (const u16*)d_in[4];
  const u16* wg = (const u16*)d_in[6];
  const u16* wu = (const u16*)d_in[7];
  const u16* wd = (const u16*)d_in[8];
  const u16* cc = (const u16*)d_in[9];
  const u16* wmod = (const u16*)d_in[10];
  char* ws = (char*)d_ws;
  k1_prep<<<600, 256, 0, stream>>>(wq, wk, wv, wo, wg, wu, wd, cc, wmod, ws);
  k2_qkv<<<376, 512, 0, stream>>>(x, wg, wu, ws);
  k3_attn<<<256, 512, 0, stream>>>(ws);
  k46_gemm<32, 8, false><<<256, 256, 0, stream>>>(x, ws + WS_OF, ws + WS_WOF, ws, (u16*)(ws + WS_X1));
  k5_ffn<<<256, 256, 0, stream>>>(ws);
  k46_gemm<112, 10, true><<<256, 256, 0, stream>>>((const u16*)(ws + WS_X1), ws + WS_HHF, ws + WS_WDF, ws, (u16*)d_out);
}
```

```cpp
#include <hip/hip_runtime.h>
#include <hip/hip_bf16.h>
#include <stdint.h>

#define DEV __device__ __forceinline__
typedef __bf16 bf16x8 __attribute__((ext_vector_type(8)));
typedef __bf16 bf16x2 __attribute__((ext_vector_type(2)));
typedef float f32x16 __attribute__((ext_vector_type(16)));
typedef float f32x4 __attribute__((ext_vector_type(4)));
typedef float f32x2 __attribute__((ext_vector_type(2)));
typedef unsigned u32x4 __attribute__((ext_vector_type(4)));
typedef unsigned u32x2 __attribute__((ext_vector_type(2)));
typedef unsigned short u16;

#define WS_MODP 0x0u
#define WS_MODF 0xC000u
#define WS_STAT 0x10000u
#define WS_WKVF 0x30000u
#define WS_KF 0x40000u
#define WS_VF 0x50000u
#define WS_WQF 0x80000u
#define WS_WOF 0x100000u
#define WS_WGUF 0x200000u
#define WS_WDF 0x600000u
#define WS_QF 0x800000u
#define WS_OF 0xA00000u
#define WS_X1 0xC00000u
#define WS_YF 0xE00000u
#define WS_HHF 0x1000000u

#define TS(k, slot)
DEV unsigned pack2(float a, float b) {
  f32x2 v = {a, b};
  bf16x2 r = __builtin_convertvector(v, bf16x2);
  return __builtin_bit_cast(unsigned, r);
}
DEV float bflo(unsigned u) { return __uint_as_float(u << 16); }
DEV float bfhi(unsigned u) { return __uint_as_float(u & 0xffff0000u); }
DEV f32x16 mfma(u32x4 a, u32x4 b, f32x16 c) {
  return __builtin_amdgcn_mfma_f32_32x32x16_bf16(__builtin_bit_cast(bf16x8, a), __builtin_bit_cast(bf16x8, b), c, 0, 0, 0);
}
template <int CTRL> DEV float dppf(float v) {
  return __builtin_bit_cast(float, __builtin_amdgcn_update_dpp(0, __builtin_bit_cast(int, v), CTRL, 0xf, 0xf, true));
}
DEV float red4(float v) { v += dppf<0xB1>(v); v += dppf<0x4E>(v); return v; }
DEV float red8(float v) { v = red4(v); v += dppf<0x141>(v); return v; }
DEV float red16(float v) { v = red8(v); v += dppf<0x140>(v); return v; }
DEV float rl(float v, int l) { return __builtin_bit_cast(float, __builtin_amdgcn_readlane(__builtin_bit_cast(int, v), l)); }
DEV float wave_sum(float v) { v = red16(v); return (rl(v, 0) + rl(v, 16)) + (rl(v, 32) + rl(v, 48)); }
#define GLD(dst, voff, sptr, imm) \
  asm volatile("global_load_dwordx4 %0, %1, %2 offset:%3" : "=v"(dst) : "v"(voff), "s"(sptr), "n"(imm) : "memory")
#define GLD1(dst, voff, sptr, imm) \
  asm volatile("global_load_dword %0, %1, %2 offset:%3" : "=v"(dst) : "v"(voff), "s"(sptr), "n"(imm) : "memory")
DEV void dma16(unsigned voff, const char* sptr, unsigned ldsoff) {
  asm volatile("s_mov_b32 m0, %2\n\ts_nop 0\n\tglobal_load_lds_dwordx4 %0, %1" ::"v"(voff), "s"(sptr), "s"(ldsoff) : "memory");
}
DEV f32x16 zero16() { f32x16 z; for (int i = 0; i < 16; ++i) z[i] = 0.f; return z; }

__global__ __launch_bounds__(256) void k1_prep(const u16* __restrict__ wq, const u16* __restrict__ wk, const u16* __restrict__ wv,
                                               const u16* __restrict__ wo, const u16* __restrict__ wg, const u16* __restrict__ wu,
                                               const u16* __restrict__ wd, const u16* __restrict__ cc, const u16* __restrict__ wmod,
                                               char* __restrict__ ws) {
  __shared__ __attribute__((aligned(16))) u16 tile[64 * 136];
  const int tid = threadIdx.x, lane = tid & 63, w = tid >> 6;
  int b = blockIdx.x;
  TS(0, 0);
  if (b < 192) {
    const int cbk = b % 48, kq = b / 48;
    const int c8 = tid & 7, kg = tid >> 3;
    const int k0 = kq * 128 + kg * 4;
    u32x4 wr[4];
#pragma unroll
    for (int i = 0; i < 4; ++i) wr[i] = *(const u32x4*)(wmod + (size_t)(k0 + i) * 3072 + cbk * 64 + c8 * 8);
    u32x2 cr = *(const u32x2*)(cc + k0);
    float cv[4] = {bflo(cr[0]), bfhi(cr[0]), bflo(cr[1]), bfhi(cr[1])};
    float acc[8];
#pragma unroll
    for (int j = 0; j < 8; ++j) acc[j] = 0.f;
#pragma unroll
    for (int i = 0; i < 4; ++i) {
      float s = cv[i] / (1.f + __expf(-cv[i]));
#pragma unroll
      for (int j = 0; j < 4; ++j) { acc[2 * j] += s * bflo(wr[i][j]); acc[2 * j + 1] += s * bfhi(wr[i][j]); }
    }
    float* part = (float*)tile;
#pragma unroll
    for (int j = 0; j < 8; ++j) acc[j] += dppf<0x128>(acc[j]);
    if ((lane & 15) < 8) {
      float* p = part + (w * 4 + (lane >> 4)) * 64 + c8 * 8;
      *(f32x4*)p = f32x4{acc[0], acc[1], acc[2], acc[3]};
      *(f32x4*)(p + 4) = f32x4{acc[4], acc[5], acc[6], acc[7]};
    }
    __syncthreads();
    {
      const int col = tid >> 2, seg = tid & 3;
      float s = 0.f;
#pragma unroll
      for (int i = 0; i < 4; ++i) s += part[(seg * 4 + i) * 64 + col];
      s = red4(s);
      if (seg == 0) ((float*)(ws + WS_MODP))[kq * 3072 + cbk * 64 + col] = s;
    }
    TS(0, 7);
    return;
  }
  b -= 192;
  if (b < 8) {
    const int ks = b * 4 + w, h = lane >> 5, m = lane & 31;
    const u16* src = (m < 16 ? wk : wv) + (m & 15) + (ks * 16 + h * 8) * 16;
    u32x4 o;
#pragma unroll
    for (int j = 0; j < 4; ++j) o[j] = (unsigned)src[(2 * j) * 16] | ((unsigned)src[(2 * j + 1) * 16] << 16);
    *(u32x4*)(ws + WS_WKVF + (ks * 64 + lane) * 16) = o;
    return;
  }
  b -= 8;
  const u16 *sl, *sr;
  int ld, perm, gu = 0, KS, t0, ks0;
  char* dst;
  if (b < 64) {
    const int o = b >> 5, bb = b & 31, kb = bb >> 2, nb = bb & 3;
    sl = (o ? wo : wq) + kb * 64 * 512 + nb * 128; sr = sl + 64; ld = 512; perm = o; KS = 32; t0 = nb * 4; ks0 = kb * 4;
    dst = ws + (o ? WS_WOF : WS_WQF);
  } else if (b < 64 + 224) {
    const int bb = b - 64, kb = bb / 28, fb = bb % 28;
    sl = wg + kb * 64 * 1792 + fb * 64; sr = wu + kb * 64 * 1792 + fb * 64; ld = 1792; perm = 0; gu = 1; KS = 33; t0 = fb * 4; ks0 = kb * 4;
    dst = ws + WS_WGUF;
  } else {
    const int bb = b - 288, kb = bb >> 2, nb = bb & 3;
    sl = wd + kb * 64 * 512 + nb * 128; sr = sl + 64; ld = 512; perm = 1; KS = 112; t0 = nb * 4; ks0 = kb * 4;
    dst = ws + WS_WDF;
  }
  u32x4 r[4];
#pragma unroll
  for (int i = 0; i < 4; ++i) {
    const int p = tid + 256 * i, row = p >> 4, c = p & 15;
    r[i] = *(const u32x4*)((c < 8 ? sl : sr) + (size_t)row * ld + (c & 7) * 8);
  }
#pragma unroll
  for (int i = 0; i < 4; ++i) {
    const int p = tid + 256 * i, row = p >> 4, c = p & 15;
    *(u32x4*)(tile + row * 136 + c * 8) = r[i];
  }
  __syncthreads();
  const int h = lane >> 5, m = lane & 31;
  const int ka = w * 16 + (perm ? 4 * h : 8 * h), kb2 = ka + (perm ? 8 : 4);
#pragma unroll
  for (int tt = 0; tt < 4; ++tt) {
    const int n = gu ? ((m < 16 ? 0 : 64) + tt * 16 + (m & 15)) : (tt * 32 + m);
    const u16* pa = tile + ka * 136 + n;
    const u16* pb = tile + kb2 * 136 + n;
    u32x4 o;
    o[0] = (unsigned)pa[0] | ((unsigned)pa[136] << 16);
    o[1] = (unsigned)pa[272] | ((unsigned)pa[408] << 16);
    o[2] = (unsigned)pb[0] | ((unsigned)pb[136] << 16);
    o[3] = (unsigned)pb[272] | ((unsigned)pb[408] << 16);
    *(u32x4*)(dst + ((size_t)((t0 + tt) * KS + ks0 + w) * 64 + lane) * 16) = o;
  }
  TS(0, 6);
}

DEV void split_bf(float v, unsigned& hi, unsigned& lo) {
  unsigned p = pack2(v, 0.f) & 0xffffu;
  float r = v - __uint_as_float(p << 16);
  hi = p; lo = pack2(r, 0.f) & 0xffffu;
}

template <bool iskv> DEV void qkv_body(const u16* __restrict__ x, char* __restrict__ ws, char* smem, const int rt, const int cb) {
  const int tid = threadIdx.x, lane = tid & 63;
  const int w = __builtin_amdgcn_readfirstlane(tid >> 6);
  const float* modp = (const float*)(ws + WS_MODP);
  float* sc = (float*)(smem + 33792 + 32768);
  const unsigned l16 = lane * 16;
  u32x4 xr[4];
  {
    const char* xp = (const char*)(x + (size_t)(rt * 32 + w * 4) * 512);
    GLD(xr[0], l16, xp, 0); GLD(xr[1], l16, xp, 1024); GLD(xr[2], l16, xp, 2048); GLD(xr[3], l16, xp, 3072);
  }
  float mp[8];
#pragma unroll
  for (int i = 0; i < 4; ++i) { GLD1(mp[i], tid * 4, modp + i * 3072, 0); GLD1(mp[4 + i], tid * 4, modp + i * 3072, 2048); }
  u32x4 wf[16];
  {
    const char* p = iskv ? ws + WS_WKVF + w * 4096 : ws + WS_WQF + (size_t)((cb * 4 + (w & 3)) * 32 + (w >> 2) * 16) * 1024;
    GLD(wf[0], l16, p, 0); GLD(wf[1], l16, p, 1024); GLD(wf[2], l16, p, 2048); GLD(wf[3], l16, p, 3072);
    if (!iskv) {
#pragma unroll
      for (int i = 1; i < 4; ++i) {
        const char* q = p + i * 4096;
        GLD(wf[4 * i], l16, q, 0); GLD(wf[4 * i + 1], l16, q, 1024); GLD(wf[4 * i + 2], l16, q, 2048); GLD(wf[4 * i + 3], l16, q, 3072);
      }
      asm volatile("s_waitcnt vmcnt(16)" : "+v"(xr[0]), "+v"(xr[1]), "+v"(xr[2]), "+v"(xr[3]), "+v"(mp[0]), "+v"(mp[1]), "+v"(mp[2]),
                   "+v"(mp[3]), "+v"(mp[4]), "+v"(mp[5]), "+v"(mp[6]), "+v"(mp[7]));
    } else {
      asm volatile("s_waitcnt vmcnt(4)" : "+v"(xr[0]), "+v"(xr[1]), "+v"(xr[2]), "+v"(xr[3]), "+v"(mp[0]), "+v"(mp[1]), "+v"(mp[2]),
                   "+v"(mp[3]), "+v"(mp[4]), "+v"(mp[5]), "+v"(mp[6]), "+v"(mp[7]));
    }
  }
  TS(1, 1);
  sc[tid] = (mp[0] + mp[1]) + (mp[2] + mp[3]);
  sc[512 + tid] = 1.f + ((mp[4] + mp[5]) + (mp[6] + mp[7]));
  float v[4][8];
#pragma unroll
  for (int i = 0; i < 4; ++i) {
#pragma unroll
    for (int j = 0; j < 4; ++j) { v[i][2 * j] = bflo(xr[i][j]); v[i][2 * j + 1] = bfhi(xr[i][j]); }
    float s = ((v[i][0] + v[i][1]) + (v[i][2] + v[i][3])) + ((v[i][4] + v[i][5]) + (v[i][6] + v[i][7]));
    const float mean = wave_sum(s) * (1.f / 512.f);
    float q = 0.f;
#pragma unroll
    for (int j = 0; j < 8; ++j) { v[i][j] -= mean; q += v[i][j] * v[i][j]; }
    const float r = 1.f / sqrtf(wave_sum(q) * (1.f / 512.f) + 1e-5f);
#pragma unroll
    for (int j = 0; j < 8; ++j) v[i][j] *= r;
  }
  asm volatile("s_waitcnt lgkmcnt(0)\n\ts_barrier" ::: "memory");
  {
    const f32x4 s0 = *(const f32x4*)(sc + lane * 8), s1 = *(const f32x4*)(sc + lane * 8 + 4);
    const f32x4 c0 = *(const f32x4*)(sc + 512 + lane * 8), c1 = *(const f32x4*)(sc + 512 + lane * 8 + 4);
#pragma unroll
    for (int i = 0; i < 4; ++i) {
      u32x4 o;
      o[0] = pack2(v[i][0] * c0[0] + s0[0], v[i][1] * c0[1] + s0[1]);
      o[1] = pack2(v[i][2] * c0[2] + s0[2], v[i][3] * c0[3] + s0[3]);
      o[2] = pack2(v[i][4] * c1[0] + s1[0], v[i][5] * c1[1] + s1[1]);
      o[3] = pack2(v[i][6] * c1[2] + s1[2], v[i][7] * c1[3] + s1[3]);
      *(u32x4*)(smem + lane * 528 + (w * 4 + i) * 16) = o;
    }
  }
  asm volatile("s_waitcnt vmcnt(0)" : "+v"(wf[0]), "+v"(wf[1]), "+v"(wf[2]), "+v"(wf[3]));
  if (!iskv)
    asm volatile("" : "+v"(wf[4]), "+v"(wf[5]), "+v"(wf[6]), "+v"(wf[7]), "+v"(wf[8]), "+v"(wf[9]), "+v"(wf[10]), "+v"(wf[11]),
                 "+v"(wf[12]), "+v"(wf[13]), "+v"(wf[14]), "+v"(wf[15]));
  if (iskv && rt < 6) {
    const int i = rt * 512 + tid;
    ((float*)(ws + WS_MODF))[i] = (modp[i] + modp[3072 + i]) + (modp[6144 + i] + modp[9216 + i]);
  }
  __syncthreads();
  TS(1, 2);
  const char* hb = smem + (lane >> 5) * 528 + (lane & 31) * 16;
  char* red = smem + 33792;
  f32x16 acc = zero16();
  if (iskv) {
#pragma unroll
    for (int i = 0; i < 4; ++i) acc = mfma(wf[i], *(const u32x4*)(hb + (w * 4 + i) * 1056), acc);
#pragma unroll
    for (int q = 0; q < 4; ++q)
      *(f32x4*)(red + (w * 4 + q) * 1024 + lane * 16) = f32x4{acc[4 * q], acc[4 * q + 1], acc[4 * q + 2], acc[4 * q + 3]};
    __syncthreads();
    if (tid < 256) {
      const int q = w;
      f32x4 s = {0, 0, 0, 0};
#pragma unroll
      for (int p = 0; p < 8; ++p) s += *(const f32x4*)(red + (p * 4 + q) * 1024 + lane * 16);
      const unsigned p0 = pack2(s[0], s[1]), p1 = pack2(s[2], s[3]);
      if (q < 2) {
        *(u32x2*)(ws + WS_KF + rt * 1024 + lane * 16 + q * 8) = u32x2{p0, p1};
      } else {
        const int t = lane & 31, h = lane >> 5;
        const int d0 = 8 * (q - 2) + 4 * h;
        u16* vp = (u16*)(ws + WS_VF + rt * 1024 + (t >> 4) * 512 + ((t >> 2) & 1) * 256 + d0 * 16) + (((t >> 3) & 1) * 4 + (t & 3));
        vp[0] = (u16)p0; vp[8] = (u16)(p0 >> 16); vp[16] = (u16)p1; vp[24] = (u16)(p1 >> 16);
      }
    }
  } else {
    const int kh = w >> 2, ct = w & 3;
#pragma unroll
    for (int i = 0; i < 16; ++i) acc = mfma(wf[i], *(const u32x4*)(hb + (kh * 16 + i) * 1056), acc);
    f32x4 k0, k1, g0, g1;
    if (kh == 0) {
      k0 = f32x4{acc[0], acc[1], acc[2], acc[3]}; k1 = f32x4{acc[4], acc[5], acc[6], acc[7]};
      g0 = f32x4{acc[8], acc[9], acc[10], acc[11]}; g1 = f32x4{acc[12], acc[13], acc[14], acc[15]};
    } else {
      g0 = f32x4{acc[0], acc[1], acc[2], acc[3]}; g1 = f32x4{acc[4], acc[5], acc[6], acc[7]};
      k0 = f32x4{acc[8], acc[9], acc[10], acc[11]}; k1 = f32x4{acc[12], acc[13], acc[14], acc[15]};
    }
    *(f32x4*)(red + w * 2048 + lane * 16) = g0;
    *(f32x4*)(red + w * 2048 + 1024 + lane * 16) = g1;
    __syncthreads();
    k0 += *(const f32x4*)(red + (w ^ 4) * 2048 + lane * 16);
    k1 += *(const f32x4*)(red + (w ^ 4) * 2048 + 1024 + lane * 16);
    const float sc = 0.25f * 1.4426950408889634f;
    k0 *= sc; k1 *= sc;
    u32x4 o = {pack2(k0[0], k0[1]), pack2(k0[2], k0[3]), pack2(k1[0], k1[1]), pack2(k1[2], k1[3])};
    const int head = (cb * 4 + ct) * 2 + kh;
    *(u32x4*)(ws + WS_QF + ((size_t)(rt * 32 + head) * 64 + lane) * 16) = o;
  }
}

__global__ __launch_bounds__(512, 4) void k2_qkv(const u16* __restrict__ x, const u16* __restrict__ wg, const u16* __restrict__ wu,
                                                 char* __restrict__ ws) {
  __shared__ __attribute__((aligned(16))) char smem[33792 + 32768 + 4096];
  const int tid = threadIdx.x, lane = tid & 63;
  const int w = __builtin_amdgcn_readfirstlane(tid >> 6);
  const float* modp = (const float*)(ws + WS_MODP);
  float* sc = (float*)(smem + 33792 + 32768);
  int b = blockIdx.x;
  TS(1, 0);
  if (b < 56) {
    const int up = b >= 28, f0 = (b % 28) * 64;
    const int c8 = tid & 7, kg = tid >> 3;
    const u16* src = (up ? wu : wg) + (size_t)(kg * 8) * 1792 + f0 + c8 * 8;
    u32x4 wr[8];
#pragma unroll
    for (int i = 0; i < 8; ++i) wr[i] = *(const u32x4*)(src + i * 1792);
    {
      const float* mp = modp + 1536 + tid;
      sc[tid] = (mp[0] + mp[3072]) + (mp[6144] + mp[9216]);
      sc[512 + tid] = 1.f + ((mp[512] + mp[3584]) + (mp[6656] + mp[9728]));
    }
    __syncthreads();
    const f32x4 s0 = *(const f32x4*)(sc + kg * 8), s1 = *(const f32x4*)(sc + kg * 8 + 4);
    const f32x4 c0 = *(const f32x4*)(sc + 512 + kg * 8), c1 = *(const f32x4*)(sc + 512 + kg * 8 + 4);
    float au[8], ab[8];
#pragma unroll
    for (int j = 0; j < 8; ++j) au[j] = ab[j] = 0.f;
#pragma unroll
    for (int i = 0; i < 8; ++i) {
      const float c = i < 4 ? c0[i & 3] : c1[i & 3], s = i < 4 ? s0[i & 3] : s1[i & 3];
#pragma unroll
      for (int j = 0; j < 4; ++j) {
        const float lo = bflo(wr[i][j]), hi = bfhi(wr[i][j]);
        au[2 * j] += c * lo; au[2 * j + 1] += c * hi; ab[2 * j] += s * lo; ab[2 * j + 1] += s * hi;
      }
    }
#pragma unroll
    for (int j = 0; j < 8; ++j) { au[j] += dppf<0x128>(au[j]); ab[j] += dppf<0x128>(ab[j]); }
    float* part = (float*)smem;
    if ((lane & 15) < 8) {
      float* p = part + (w * 4 + (lane >> 4)) * 128 + c8 * 16;
      *(f32x4*)p = f32x4{au[0], au[1], au[2], au[3]};
      *(f32x4*)(p + 4) = f32x4{au[4], au[5], au[6], au[7]};
      *(f32x4*)(p + 8) = f32x4{ab[0], ab[1], ab[2], ab[3]};
      *(f32x4*)(p + 12) = f32x4{ab[4], ab[5], ab[6], ab[7]};
    }
    __syncthreads();
    const int col = tid >> 3, seg = tid & 7;
    float u = 0.f, bb = 0.f;
#pragma unroll
    for (int i = 0; i < 4; ++i) {
      const float* p = part + (seg * 4 + i) * 128 + (col >> 3) * 16 + (col & 7);
      u += p[0]; bb += p[8];
    }
    u = red8(u); bb = red8(bb);
    if (seg < 2) {
      unsigned uh, ul, bh, bl;
      split_bf(u, uh, ul); split_bf(bb, bh, bl);
      u32x4 o = {uh | (ul << 16), uh | (ul << 16), bh | (bl << 16), bh | (bl << 16)};
      if (seg) o = u32x4{0, 0, 0, 0};
      const int f = f0 + col, t = f >> 4, m = (f & 15) + 16 * up + 32 * seg;
      *(u32x4*)(ws + WS_WGUF + ((size_t)(t * 33 + 32) * 64 + m) * 16) = o;
    }
    TS(1, 7);
    return;
  }
  b -= 56;
  if (b < 64) qkv_body<true>(x, ws, smem, b, 0);
  else qkv_body<false>(x, ws, smem, b & 63, (b >> 6) - 1);
  TS(1, 6);
}

DEV float xhalf_max(float m) { return fmaxf(m, __shfl_xor(m, 32)); }
DEV float max16(const f32x16& s) {
  float a = fmaxf(fmaxf(s[0], s[1]), s[2]), b = fmaxf(fmaxf(s[3], s[4]), s[5]), c = fmaxf(fmaxf(s[6], s[7]), s[8]);
  float d = fmaxf(fmaxf(s[9], s[10]), s[11]), e = fmaxf(fmaxf(s[12], s[13]), s[14]);
  return fmaxf(fmaxf(fmaxf(a, b), c), fmaxf(fmaxf(d, e), s[15]));
}
#define ATT_LOOP \
  "1:\n\tv_exp_f32 v0, v0\n\tv_exp_f32 v1, v1\n\tv_exp_f32 v2, v2\n\t" \
  "v_exp_f32 v3, v3\n\tv_mfma_f32_32x32x16_bf16 v[48:63], v[88:91], v[96:99], v[48:63]\n\ts_waitcnt lgkmcnt(0)\n\tds_read_b128 v[68:71], v100 offset:2048\n\t" \
  "s_mov_b64 exec, %[msk]\n\tds_read_b128 v[84:87], v101 offset:1024\n\tds_read_b128 v[88:91], v101 offset:1536\n\ts_mov_b64 exec, -1\n\t" \
  "v_cvt_pk_bf16_f32 v92, v0, v1\n\tv_exp_f32 v4, v4\n\tv_exp_f32 v5, v5\n\tv_mfma_f32_32x32x16_bf16 v[16:31], v[72:75], v[64:67], v[32:47]\n\t" \
  "v_cvt_pk_bf16_f32 v93, v2, v3\n\tv_exp_f32 v6, v6\n\tv_exp_f32 v7, v7\n\tv_cvt_pk_bf16_f32 v94, v4, v5\n\t" \
  "v_exp_f32 v8, v8\n\tv_exp_f32 v9, v9\n\tv_cvt_pk_bf16_f32 v95, v6, v7\n\tv_exp_f32 v10, v10\n\t" \
  "v_exp_f32 v11, v11\n\tv_mfma_f32_32x32x16_bf16 v[48:63], v[76:79], v[92:95], v[48:63]\n\tv_cvt_pk_bf16_f32 v96, v8, v9\n\tv_exp_f32 v12, v12\n\t" \
  "v_exp_f32 v13, v13\n\tv_cvt_pk_bf16_f32 v97, v10, v11\n\tv_exp_f32 v14, v14\n\tv_exp_f32 v15, v15\n\t" \
  "v_cvt_pk_bf16_f32 v98, v12, v13\n\tv_cvt_pk_bf16_f32 v99, v14, v15\n\tv_exp_f32 v16, v16\n\tv_exp_f32 v17, v17\n\t" \
  "v_exp_f32 v18, v18\n\tv_exp_f32 v19, v19\n\tv_mfma_f32_32x32x16_bf16 v[48:63], v[80:83], v[96:99], v[48:63]\n\ts_waitcnt lgkmcnt(0)\n\t" \
  "ds_read_b128 v[72:75], v100 offset:3072\n\ts_mov_b64 exec, %[msk]\n\tds_read_b128 v[76:79], v101 offset:2048\n\tds_read_b128 v[80:83], v101 offset:2560\n\t" \
  "s_mov_b64 exec, -1\n\tv_cvt_pk_bf16_f32 v92, v16, v17\n\tv_exp_f32 v20, v20\n\tv_exp_f32 v21, v21\n\t" \
  "v_mfma_f32_32x32x16_bf16 v[0:15], v[68:71], v[64:67], v[32:47]\n\tv_cvt_pk_bf16_f32 v93, v18, v19\n\tv_exp_f32 v22, v22\n\tv_exp_f32 v23, v23\n\t" \
  "v_cvt_pk_bf16_f32 v94, v20, v21\n\tv_exp_f32 v24, v24\n\tv_exp_f32 v25, v25\n\tv_cvt_pk_bf16_f32 v95, v22, v23\n\t" \
  "v_exp_f32 v26, v26\n\tv_exp_f32 v27, v27\n\tv_mfma_f32_32x32x16_bf16 v[48:63], v[84:87], v[92:95], v[48:63]\n\tv_add_u32 v100, 0x800, v100\n\t" \
  "v_add_u32 v101, 0x800, v101\n\ts_sub_u32 %[n], %[n], 1\n\ts_cmp_lg_u32 %[n], 0\n\tv_cvt_pk_bf16_f32 v96, v24, v25\n\t" \
  "v_exp_f32 v28, v28\n\tv_exp_f32 v29, v29\n\tv_cvt_pk_bf16_f32 v97, v26, v27\n\tv_exp_f32 v30, v30\n\t" \
  "v_exp_f32 v31, v31\n\tv_cvt_pk_bf16_f32 v98, v28, v29\n\tv_cvt_pk_bf16_f32 v99, v30, v31\n\ts_cbranch_scc1 1b\n\t" \
  "s_nop 1\n\tv_mfma_f32_32x32x16_bf16 v[48:63], v[88:91], v[96:99], v[48:63]\n\ts_nop 7\n\tv_mov_b32 v96, 0\n\t" \
  "v_mov_b32 v97, 0\n\tv_mov_b32 v98, 0\n\tv_mov_b32 v99, 0\n\ts_waitcnt lgkmcnt(0)\n\t" \
  "s_nop 15\n\t\n\t" \
  ""
#define LDK(dst, addr, off) asm volatile("ds_read_b128 %0, %1 offset:" #off : "=v"(dst) : "v"(addr))
#define LDV(d0, d1, addr, off0, off1)                                                                                   \
  asm volatile("s_mov_b64 exec, %3\n\tds_read_b128 %0, %2 offset:" #off0 "\n\tds_read_b128 %1, %2 offset:" #off1        \
               "\n\ts_mov_b64 exec, -1"                                                                                 \
               : "+v"(d0), "+v"(d1)                                                                                     \
               : "v"(addr), "s"(vmask))
#define LWAIT(a, b, c) asm volatile("s_waitcnt lgkmcnt(0)" : "+v"(a), "+v"(b), "+v"(c))

__global__ __launch_bounds__(1024, 4) void k3_attn(char* __restrict__ ws) {
  __shared__ __attribute__((aligned(16))) char smem[131072 + 4096];
  const int tid = threadIdx.x, lane = tid & 63;
  const int w = __builtin_amdgcn_readfirstlane(tid >> 6);
  const int rt = blockIdx.x & 63, hg = blockIdx.x >> 6;
  TS(2, 0);
  const int kh = w >> 3;
  const unsigned sbase = (unsigned)(uintptr_t)smem;
  const int head = hg * 8 + (w & 7);
  u32x4 q;
  {
    const unsigned l16 = lane * 16;
    GLD(q, l16, ws + WS_QF + (size_t)(rt * 32 + head) * 1024, 0);
    const char* kp = ws + WS_KF + kh * 32768 + (w & 7) * 1024;
    const unsigned lb = sbase + kh * 32768 + (w & 7) * 1024;
#pragma unroll
    for (int i = 0; i < 4; ++i) {
      dma16(l16, kp + i * 8192, lb + i * 8192);
      dma16(l16, kp + i * 8192 + 65536, lb + i * 8192 + 65536);
    }
  }
  const unsigned long long vmask = 0x0000ffff0000ffffull;
  const unsigned kbase = sbase + kh * 32768 + lane * 16;
  const unsigned vbase = sbase + 65536 + kh * 32768 + (lane >> 5) * 256 + (lane & 15) * 16;
  const u32x4 ones = {0x3f803f80u, 0x3f803f80u, 0x3f803f80u, 0x3f803f80u};
  u32x4 va0 = ones, va1 = ones, vb0 = ones, vb1 = ones, ka, kb;
  asm volatile("s_waitcnt vmcnt(6)\n\ts_barrier" : "+v"(q)::"memory");
  TS(2, 1);
  f32x16 o;
  float off;
  for (int attempt = 0; attempt < 2; ++attempt) {
    float m = -3.0e38f;
    const int ntm = attempt ? 32 : 1;
    for (int t = 0; t < ntm; ++t) {
      const unsigned a = kbase + t * 1024;
      LDK(ka, a, 0);
      LWAIT(ka, va0, va1);
      m = fmaxf(m, max16(mfma(ka, q, zero16())));
    }
    m = xhalf_max(m);
    const float thr = attempt ? __builtin_inff() : 5.9604645e-8f;
    off = m + (attempt ? 0.f : 40.f);
    f32x16 c;
#pragma unroll
    for (int i = 0; i < 16; ++i) c[i] = -off;
    o = zero16();
    unsigned kad = kbase, vad = vbase;
    LDK(ka, kad, 0);
    LDK(kb, kad, 1024);
    LDV(va0, va1, vad, 0, 512);
    asm volatile("s_waitcnt lgkmcnt(0)" : "+v"(ka), "+v"(kb), "+v"(va0), "+v"(va1));
    f32x16 s0 = mfma(ka, q, c);
    u32x4 pz = {0, 0, 0, 0};
    for (int seg = 0; seg < 5; ++seg) {
      if (seg == 2 && attempt == 0) asm volatile("s_waitcnt vmcnt(0)\n\ts_barrier" ::: "memory");
      if (seg) {
        if (__any(o[8] > thr)) {
          const int e = __builtin_amdgcn_frexp_expf(o[8]) + 40;
          const float f = (float)e;
#pragma unroll
          for (int i = 0; i < 9; ++i) o[i] = __builtin_amdgcn_ldexpf(o[i], -e);
          off += f;
#pragma unroll
          for (int i = 0; i < 16; ++i) { c[i] = -off; s0[i] -= f; }
        }
      }
      int np = seg ? (1 << (seg - 1)) : 1;
      asm volatile("s_nop 15\n\t" ATT_LOOP
                   : "+{v[0:15]}"(s0), "+{v[48:63]}"(o), "+{v[72:75]}"(kb), "+{v[76:79]}"(va0), "+{v[80:83]}"(va1),
                     "+{v[84:87]}"(vb0), "+{v[88:91]}"(vb1), "+{v[96:99]}"(pz), "+{v100}"(kad), "+{v101}"(vad), [n] "+s"(np)
                   : "{v[32:47]}"(c), "{v[64:67]}"(q), [msk] "s"(vmask)
                   : "v16", "v17", "v18", "v19", "v20", "v21", "v22", "v23", "v24", "v25", "v26", "v27", "v28", "v29", "v30", "v31",
                     "v68", "v69", "v70", "v71", "v92", "v93", "v94", "v95", "scc", "memory");
    }
    if (attempt) break;
    float a = 0.f, s = o[8];
#pragma unroll
    for (int i = 0; i < 8; ++i) { a = fmaxf(a, fabsf(o[i])); s += o[i] * 0.f; }
    const bool ok = (s < 3.0e38f) && (a >= 7.8886091e-31f);
    if (!__any(!ok)) break;
  }
  TS(2, 2);
  __syncthreads();
  char* ex = smem + (w & 7) * 3072 + lane * 16;
  if (kh) {
    *(f32x4*)(ex) = f32x4{o[0], o[1], o[2], o[3]};
    *(f32x4*)(ex + 1024) = f32x4{o[4], o[5], o[6], o[7]};
    *(f32x2*)(ex + 2048) = f32x2{o[8], off};
  }
  __syncthreads();
  if (!kh) {
    const f32x4 g0 = *(const f32x4*)(ex), g1 = *(const f32x4*)(ex + 1024);
    const f32x2 g2 = *(const f32x2*)(ex + 2048);
    const float m = fmaxf(off, g2[1]);
    float fk = __builtin_amdgcn_exp2f(off - m), fg = __builtin_amdgcn_exp2f(g2[1] - m);
    const float inv = 1.f / (o[8] * fk + g2[0] * fg);
    fk *= inv; fg *= inv;
    const f32x4 k0 = f32x4{o[0], o[1], o[2], o[3]} * fk + g0 * fg, k1 = f32x4{o[4], o[5], o[6], o[7]} * fk + g1 * fg;
    const u32x4 ov = {pack2(k0[0], k0[1]), pack2(k0[2], k0[3]), pack2(k1[0], k1[1]), pack2(k1[2], k1[3])};
    *(u32x4*)(ws + WS_OF + ((size_t)(rt * 32 + head) * 64 + lane) * 16) = ov;
  }
  TS(2, 6);
}

template <int KS, int DEPTH, bool FINAL>
__global__ __launch_bounds__(256, 1) void k46_gemm(const u16* __restrict__ res, const char* __restrict__ af, const char* __restrict__ wf,
                                                   char* __restrict__ ws, u16* __restrict__ out) {
  __shared__ __attribute__((aligned(16))) float red[4 * 64 * 68];
  const int tid = threadIdx.x, lane = tid & 63;
  const int w = __builtin_amdgcn_readfirstlane(tid >> 6);
  const int b = blockIdx.x;
  const int rb = (b & 7) * 4 + ((b >> 3) & 3), cbk = b >> 5;
  constexpr int N = KS / 4;
  TS(FINAL ? 5 : 3, 0);
  const unsigned l16 = lane * 16;
  const char* ap = af + (size_t)(rb * 2 * KS + w * N) * 1024;
  const char* wp = wf + (size_t)(cbk * 2 * KS + w * N) * 1024;
  u32x4 fa[DEPTH][2], fw[DEPTH][2];
#define LD4(s, k)                                                                                     \
  {                                                                                                   \
    GLD(fa[s][0], l16, ap + ((k) >> 2) * 4096, ((k) & 3) * 1024);                                     \
    GLD(fw[s][0], l16, wp + ((k) >> 2) * 4096, ((k) & 3) * 1024);                                     \
    GLD(fa[s][1], l16, ap + KS * 1024 + ((k) >> 2) * 4096, ((k) & 3) * 1024);                         \
    GLD(fw[s][1], l16, wp + KS * 1024 + ((k) >> 2) * 4096, ((k) & 3) * 1024);                         \
  }
#pragma unroll
  for (int i = 0; i < DEPTH; ++i) LD4(i, i)
  const int cg = tid & 7, r0 = tid >> 3;
  const int col = cbk * 64 + cg * 8;
  const float* modf = (const float*)(ws + WS_MODF);
  u32x4 xr[2], g0u, g1u, c0u, c1u;
  {
    const char* rp = (const char*)(res + (size_t)(rb * 64) * 512 + cbk * 64);
    const unsigned ro = r0 * 1024 + cg * 16;
    GLD(xr[0], ro, rp, 0); GLD(xr[1], ro, rp + 32768, 0);
    const unsigned mo = cg * 32;
    const char* gp = (const char*)(modf + (FINAL ? 2560 : 1024) + cbk * 64);
    GLD(g0u, mo, gp, 0); GLD(g1u, mo, gp, 16);
    const char* cp = (const char*)(modf + 2048 + cbk * 64);
    GLD(c0u, mo, cp, 0); GLD(c1u, mo, cp, 16);
  }
  f32x16 acc[2][2];
  acc[0][0] = acc[0][1] = acc[1][0] = acc[1][1] = zero16();
#pragma unroll
  for (int k = 0; k < N; ++k) {
    const int s = k % DEPTH;
    const int after = 4 * ((k + DEPTH < N ? k + DEPTH : N) - k - 1) + (k < DEPTH ? 6 : 0);
    asm volatile("s_waitcnt vmcnt(%4)" : "+v"(fa[s][0]), "+v"(fa[s][1]), "+v"(fw[s][0]), "+v"(fw[s][1]) : "n"(after));
    if (k == 0) TS(FINAL ? 5 : 3, 1);
    acc[0][0] = mfma(fw[s][0], fa[s][0], acc[0][0]);
    acc[0][1] = mfma(fw[s][0], fa[s][1], acc[0][1]);
    acc[1][0] = mfma(fw[s][1], fa[s][0], acc[1][0]);
    acc[1][1] = mfma(fw[s][1], fa[s][1], acc[1][1]);
    if (k + DEPTH < N) LD4(s, k + DEPTH)
  }
  asm volatile("s_waitcnt vmcnt(0)" : "+v"(xr[0]), "+v"(xr[1]), "+v"(g0u), "+v"(g1u), "+v"(c0u), "+v"(c1u));
  TS(FINAL ? 5 : 3, 2);
  const f32x4 g0 = __builtin_bit_cast(f32x4, g0u), g1 = __builtin_bit_cast(f32x4, g1u);
  const f32x4 c0 = __builtin_bit_cast(f32x4, c0u) + 1.f, c1 = __builtin_bit_cast(f32x4, c1u) + 1.f;
  {
    float* rp = red + w * (64 * 68) + (lane & 31) * 68 + (lane >> 5) * 4;
#pragma unroll
    for (int i = 0; i < 2; ++i)
#pragma unroll
      for (int j = 0; j < 2; ++j)
#pragma unroll
        for (int q = 0; q < 4; ++q)
          *(f32x4*)(rp + j * 32 * 68 + i * 32 + q * 8) =
              f32x4{acc[i][j][4 * q], acc[i][j][4 * q + 1], acc[i][j][4 * q + 2], acc[i][j][4 * q + 3]};
  }
  __syncthreads();
#pragma unroll
  for (int e = 0; e < 2; ++e) {
    const int r = r0 + 32 * e, row = rb * 64 + r;
    f32x4 s0 = {0, 0, 0, 0}, s1 = s0;
#pragma unroll
    for (int p = 0; p < 4; ++p) {
      const float* rp = red + p * (64 * 68) + r * 68 + cg * 8;
      s0 += *(const f32x4*)rp; s1 += *(const f32x4*)(rp + 4);
    }
    const f32x4 x0 = {bflo(xr[e][0]), bfhi(xr[e][0]), bflo(xr[e][1]), bfhi(xr[e][1])};
    const f32x4 x1 = {bflo(xr[e][2]), bfhi(xr[e][2]), bflo(xr[e][3]), bfhi(xr[e][3])};
    s0 = x0 + g0 * s0; s1 = x1 + g1 * s1;
    const u32x4 o = {pack2(s0[0], s0[1]), pack2(s0[2], s0[3]), pack2(s1[0], s1[1]), pack2(s1[2], s1[3])};
    *(u32x4*)(out + (size_t)row * 512 + col) = o;
    if (!FINAL) {
      const f32x4 y0 = s0 * c0, y1 = s1 * c1;
      const u32x4 y = {pack2(y0[0], y0[1]), pack2(y0[2], y0[3]), pack2(y1[0], y1[1]), pack2(y1[2], y1[3])};
      *(u32x4*)(ws + WS_YF + (size_t)((row >> 5) * 32 + (col >> 4)) * 1024 + ((col >> 3) & 1) * 512 + (row & 31) * 16) = y;
      const float mean = red8(((s0[0] + s0[1]) + (s0[2] + s0[3])) + ((s1[0] + s1[1]) + (s1[2] + s1[3]))) * (1.f / 64.f);
      s0 -= mean; s1 -= mean;
      s0 *= s0; s1 *= s1;
      const float m2 = red8(((s0[0] + s0[1]) + (s0[2] + s0[3])) + ((s1[0] + s1[1]) + (s1[2] + s1[3])));
      if (cg == 0) *(f32x2*)(ws + WS_STAT + (size_t)(row * 8 + cbk) * 8) = f32x2{mean, m2};
    }
  }
  TS(FINAL ? 5 : 3, 6);
}

__global__ __launch_bounds__(256, 1) void k5_ffn(char* __restrict__ ws) {
  __shared__ __attribute__((aligned(16))) char smem[5 * 28672 + 7168];
  const int tid = threadIdx.x, lane = tid & 63;
  const int w = __builtin_amdgcn_readfirstlane(tid >> 6);
  const int b = blockIdx.x, xc = b & 7, idx = b >> 3;
  const int rb = (xc & 1) * 8 + (idx & 7), cbk = (xc >> 1) * 4 + (idx >> 3);
  const int rt = rb * 4 + w, t0 = cbk * 7;
  TS(4, 0);
  const unsigned sbase = (unsigned)(uintptr_t)smem;
  const unsigned l16 = lane * 16;
  const char* wsrc = ws + WS_WGUF + (size_t)t0 * 33 * 1024;
  const char* ysrc = ws + WS_YF + (size_t)rt * 32 * 1024;
  u32x4 st[4], y[32];
  {
    const char* sp = ws + WS_STAT + (size_t)(rt * 32) * 64;
    const unsigned so = (lane & 31) * 64;
    GLD(st[0], so, sp, 0); GLD(st[1], so, sp, 16); GLD(st[2], so, sp, 32); GLD(st[3], so, sp, 48);
    const int i2 = w + 4 < 7 ? w + 4 : 6;
    dma16(l16, wsrc + (w * 33 + 32) * 1024, sbase + 5 * 28672 + w * 1024);
    dma16(l16, wsrc + (i2 * 33 + 32) * 1024, sbase + 5 * 28672 + i2 * 1024);
  }
#define ISSUE(c)                                                                                             \
  {                                                                                                          \
    _Pragma("unroll") for (int i = 0; i < 7; ++i)                                                            \
        dma16(l16, wsrc + (i * 33 + 4 * (c) + w) * 1024, sbase + ((c) % 5) * 28672 + (w * 7 + i) * 1024);    \
    const char* yp = ysrc + (c) * 4096;                                                                      \
    GLD(y[4 * (c)], l16, yp, 0); GLD(y[4 * (c) + 1], l16, yp, 1024);                                         \
    GLD(y[4 * (c) + 2], l16, yp, 2048); GLD(y[4 * (c) + 3], l16, yp, 3072);                                  \
  }
#define WAITB(n, c)                                                                                          \
  asm volatile("s_waitcnt vmcnt(" #n ")\n\ts_barrier"                                                        \
               : "+v"(y[4 * (c)]), "+v"(y[4 * (c) + 1]), "+v"(y[4 * (c) + 2]), "+v"(y[4 * (c) + 3])::"memory")
  ISSUE(0) ISSUE(1) ISSUE(2) ISSUE(3) ISSUE(4)
  f32x16 acc[7];
#pragma unroll
  for (int i = 0; i < 7; ++i) acc[i] = zero16();
  const char* lp = smem + lane * 16;
  u32x4 wc[7], wn[7];
  WAITB(44, 0);
  asm volatile("" : "+v"(st[0]), "+v"(st[1]), "+v"(st[2]), "+v"(st[3]));
#pragma unroll
  for (int i = 0; i < 7; ++i) wc[i] = *(const u32x4*)(lp + i * 1024);
  TS(4, 1);
  float r2;
  u32x4 yx;
  {
    float mb[8], m2 = 0.f, mean = 0.f;
#pragma unroll
    for (int i = 0; i < 4; ++i) {
      mb[2 * i] = __uint_as_float(st[i][0]); mb[2 * i + 1] = __uint_as_float(st[i][2]);
      m2 += __uint_as_float(st[i][1]) + __uint_as_float(st[i][3]);
      mean += mb[2 * i] + mb[2 * i + 1];
    }
    mean *= 0.125f;
    float d = 0.f;
#pragma unroll
    for (int i = 0; i < 8; ++i) d += (mb[i] - mean) * (mb[i] - mean);
    const float var = (m2 + 64.f * d) * (1.f / 512.f) + 1e-5f;
    r2 = 1.f / var;
    unsigned mh, ml, rh, rlo;
    split_bf(-mean, mh, ml); split_bf(sqrtf(var), rh, rlo);
    yx = u32x4{mh | (mh << 16), ml | (ml << 16), rh | (rh << 16), rlo | (rlo << 16)};
    if (lane >= 32) yx = u32x4{0, 0, 0, 0};
  }
#define KSTEP(g, CUR, NXT)                                                                                   \
  {                                                                                                          \
    const char* np = lp + ((g) == 31 ? 5 * 28672 : ((((g) + 1) / 4) % 5) * 28672 + (((g) + 1) % 4) * 7168);  \
    _Pragma("unroll") for (int i = 0; i < 7; ++i) NXT[i] = *(const u32x4*)(np + i * 1024);                   \
    _Pragma("unroll") for (int i = 0; i < 7; ++i) acc[i] = mfma(CUR[i], y[g], acc[i]);                       \
  }
#define CHUNK(c, PRE)                                                                                        \
  KSTEP(4 * (c), wc, wn) KSTEP(4 * (c) + 1, wn, wc) KSTEP(4 * (c) + 2, wc, wn) PRE KSTEP(4 * (c) + 3, wn, wc)
  CHUNK(0, WAITB(33, 1);)
  CHUNK(1, WAITB(22, 2); ISSUE(5))
  CHUNK(2, WAITB(22, 3); ISSUE(6))
  CHUNK(3, WAITB(22, 4); ISSUE(7))
  CHUNK(4, WAITB(22, 5);)
  CHUNK(5, WAITB(11, 6);)
  CHUNK(6, WAITB(0, 7);)
  CHUNK(7, )
  TS(4, 2);
  char* hp = ws + WS_HHF + ((size_t)(rt * 112 + t0) * 64 + lane) * 16;
#pragma unroll
  for (int i = 0; i < 7; ++i) {
    const f32x16 a = mfma(wc[i], yx, acc[i]);
    float hv[8];
#pragma unroll
    for (int j = 0; j < 8; ++j) hv[j] = fmaxf(a[j], 0.f) * r2 * a[j + 8];
    *(u32x4*)(hp + i * 1024) = u32x4{pack2(hv[0], hv[1]), pack2(hv[2], hv[3]), pack2(hv[4], hv[5]), pack2(hv[6], hv[7])};
  }
  TS(4, 6);
}

extern "C" void kernel_launch(void* const* d_in, const int* in_sizes, int n_in, void* d_out, int out_size, void* d_ws,
                              size_t ws_size, hipStream_t stream) {
  const u16* x = (const u16*)d_in[0];
  const u16* wq = (const u16*)d_in[1];
  const u16* wk = (const u16*)d_in[2];
  const u16* wv = (const u16*)d_in[3];
  const u16* wo = (const u16*)d_in[4];
  const u16* wg = (const u16*)d_in[6];
  const u16* wu = (const u16*)d_in[7];
  const u16* wd = (const u16*)d_in[8];
  const u16* cc = (const u16*)d_in[9];
  const u16* wmod = (const u16*)d_in[10];
  char* ws = (char*)d_ws;
  k1_prep<<<600, 256, 0, stream>>>(wq, wk, wv, wo, wg, wu, wd, cc, wmod, ws);
  k2_qkv<<<376, 512, 0, stream>>>(x, wg, wu, ws);
  k3_attn<<<256, 1024, 0, stream>>>(ws);
  k46_gemm<32, 8, false><<<256, 256, 0, stream>>>(x, ws + WS_OF, ws + WS_WOF, ws, (u16*)(ws + WS_X1));
  k5_ffn<<<256, 256, 0, stream>>>(ws);
  k46_gemm<112, 10, true><<<256, 256, 0, stream>>>((const u16*)(ws + WS_X1), ws + WS_HHF, ws + WS_WDF, ws, (u16*)d_out);
}
```

```cpp
#include <hip/hip_runtime.h>
#include <hip/hip_bf16.h>
#include <stdint.h>

#define DEV __device__ __forceinline__
typedef __bf16 bf16x8 __attribute__((ext_vector_type(8)));
typedef __bf16 bf16x2 __attribute__((ext_vector_type(2)));
typedef float f32x16 __attribute__((ext_vector_type(16)));
typedef float f32x4 __attribute__((ext_vector_type(4)));
typedef float f32x2 __attribute__((ext_vector_type(2)));
typedef unsigned u32x4 __attribute__((ext_vector_type(4)));
typedef unsigned u32x2 __attribute__((ext_vector_type(2)));
typedef unsigned short u16;

#define WS_MODP 0x0u
#define WS_MODF 0xC000u
#define WS_STAT 0x10000u
#define WS_WKVF 0x30000u
#define WS_KF 0x40000u
#define WS_VF 0x50000u
#define WS_WQF 0x80000u
#define WS_WOF 0x100000u
#define WS_WGUF 0x200000u
#define WS_WDF 0x600000u
#define WS_QF 0x800000u
#define WS_OF 0xA00000u
#define WS_X1 0xC00000u
#define WS_YF 0xE00000u
#define WS_HHF 0x1000000u

#define TS(k, slot)
DEV unsigned pack2(float a, float b) {
  f32x2 v = {a, b};
  bf16x2 r = __builtin_convertvector(v, bf16x2);
  return __builtin_bit_cast(unsigned, r);
}
DEV float bflo(unsigned u) { return __uint_as_float(u << 16); }
DEV float bfhi(unsigned u) { return __uint_as_float(u & 0xffff0000u); }
DEV f32x16 mfma(u32x4 a, u32x4 b, f32x16 c) {
  return __builtin_amdgcn_mfma_f32_32x32x16_bf16(__builtin_bit_cast(bf16x8, a), __builtin_bit_cast(bf16x8, b), c, 0, 0, 0);
}
template <int CTRL> DEV float dppf(float v) {
  return __builtin_bit_cast(float, __builtin_amdgcn_update_dpp(0, __builtin_bit_cast(int, v), CTRL, 0xf, 0xf, true));
}
DEV float red4(float v) { v += dppf<0xB1>(v); v += dppf<0x4E>(v); return v; }
DEV float red8(float v) { v = red4(v); v += dppf<0x141>(v); return v; }
DEV float red16(float v) { v = red8(v); v += dppf<0x140>(v); return v; }
DEV float rl(float v, int l) { return __builtin_bit_cast(float, __builtin_amdgcn_readlane(__builtin_bit_cast(int, v), l)); }
DEV float wave_sum(float v) { v = red16(v); return (rl(v, 0) + rl(v, 16)) + (rl(v, 32) + rl(v, 48)); }
#define GLD(dst, voff, sptr, imm) \
  asm volatile("global_load_dwordx4 %0, %1, %2 offset:%3" : "=v"(dst) : "v"(voff), "s"(sptr), "n"(imm) : "memory")
#define GLD1(dst, voff, sptr, imm) \
  asm volatile("global_load_dword %0, %1, %2 offset:%3" : "=v"(dst) : "v"(voff), "s"(sptr), "n"(imm) : "memory")
DEV void dma16(unsigned voff, const char* sptr, unsigned ldsoff) {
  asm volatile("s_mov_b32 m0, %2\n\ts_nop 0\n\tglobal_load_lds_dwordx4 %0, %1" ::"v"(voff), "s"(sptr), "s"(ldsoff) : "memory");
}
DEV f32x16 zero16() { f32x16 z; for (int i = 0; i < 16; ++i) z[i] = 0.f; return z; }

__global__ __launch_bounds__(256) void k1_prep(const u16* __restrict__ wq, const u16* __restrict__ wk, const u16* __restrict__ wv,
                                               const u16* __restrict__ cc, const u16* __restrict__ wmod, char* __restrict__ ws) {
  __shared__ __attribute__((aligned(16))) u16 tile[64 * 136];
  const int tid = threadIdx.x, lane = tid & 63, w = tid >> 6;
  int b = blockIdx.x;
  TS(0, 0);
  if (b < 192) {
    const int cbk = b % 48, kq = b / 48;
    const int c8 = tid & 7, kg = tid >> 3;
    const int k0 = kq * 128 + kg * 4;
    u32x4 wr[4];
#pragma unroll
    for (int i = 0; i < 4; ++i) wr[i] = *(const u32x4*)(wmod + (size_t)(k0 + i) * 3072 + cbk * 64 + c8 * 8);
    u32x2 cr = *(const u32x2*)(cc + k0);
    float cv[4] = {bflo(cr[0]), bfhi(cr[0]), bflo(cr[1]), bfhi(cr[1])};
    float acc[8];
#pragma unroll
    for (int j = 0; j < 8; ++j) acc[j] = 0.f;
#pragma unroll
    for (int i = 0; i < 4; ++i) {
      float s = cv[i] / (1.f + __expf(-cv[i]));
#pragma unroll
      for (int j = 0; j < 4; ++j) { acc[2 * j] += s * bflo(wr[i][j]); acc[2 * j + 1] += s * bfhi(wr[i][j]); }
    }
    float* part = (float*)tile;
#pragma unroll
    for (int j = 0; j < 8; ++j) acc[j] += dppf<0x128>(acc[j]);
    if ((lane & 15) < 8) {
      float* p = part + (w * 4 + (lane >> 4)) * 64 + c8 * 8;
      *(f32x4*)p = f32x4{acc[0], acc[1], acc[2], acc[3]};
      *(f32x4*)(p + 4) = f32x4{acc[4], acc[5], acc[6], acc[7]};
    }
    __syncthreads();
    {
      const int col = tid >> 2, seg = tid & 3;
      float s = 0.f;
#pragma unroll
      for (int i = 0; i < 4; ++i) s += part[(seg * 4 + i) * 64 + col];
      s = red4(s);
      if (seg == 0) ((float*)(ws + WS_MODP))[kq * 3072 + cbk * 64 + col] = s;
    }
    TS(0, 7);
    return;
  }
  b -= 192;
  if (b < 8) {
    const int ks = b * 4 + w, h = lane >> 5, m = lane & 31;
    const u16* src = (m < 16 ? wk : wv) + (m & 15) + (ks * 16 + h * 8) * 16;
    u32x4 o;
#pragma unroll
    for (int j = 0; j < 4; ++j) o[j] = (unsigned)src[(2 * j) * 16] | ((unsigned)src[(2 * j + 1) * 16] << 16);
    *(u32x4*)(ws + WS_WKVF + (ks * 64 + lane) * 16) = o;
    return;
  }
  b -= 8;
  const int kb = b >> 2, nb = b & 3;
  const u16* sl = wq + kb * 64 * 512 + nb * 128;
  u32x4 r[4];
#pragma unroll
  for (int i = 0; i < 4; ++i) {
    const int p = tid + 256 * i, row = p >> 4, c = p & 15;
    r[i] = *(const u32x4*)(sl + row * 512 + c * 8);
  }
#pragma unroll
  for (int i = 0; i < 4; ++i) {
    const int p = tid + 256 * i, row = p >> 4, c = p & 15;
    *(u32x4*)(tile + row * 136 + c * 8) = r[i];
  }
  __syncthreads();
  const int h = lane >> 5, m = lane & 31;
  const int ka = w * 16 + 8 * h, kb2 = ka + 4;
#pragma unroll
  for (int tt = 0; tt < 4; ++tt) {
    const int n = tt * 32 + m;
    const u16* pa = tile + ka * 136 + n;
    const u16* pb = tile + kb2 * 136 + n;
    u32x4 o;
    o[0] = (unsigned)pa[0] | ((unsigned)pa[136] << 16);
    o[1] = (unsigned)pa[272] | ((unsigned)pa[408] << 16);
    o[2] = (unsigned)pb[0] | ((unsigned)pb[136] << 16);
    o[3] = (unsigned)pb[272] | ((unsigned)pb[408] << 16);
    *(u32x4*)(ws + WS_WQF + ((size_t)((nb * 4 + tt) * 32 + kb * 4 + w) * 64 + lane) * 16) = o;
  }
  TS(0, 6);
}

DEV void split_bf(float v, unsigned& hi, unsigned& lo) {
  unsigned p = pack2(v, 0.f) & 0xffffu;
  float r = v - __uint_as_float(p << 16);
  hi = p; lo = pack2(r, 0.f) & 0xffffu;
}

template <bool iskv> DEV void qkv_body(const u16* __restrict__ x, char* __restrict__ ws, char* smem, const int rt, const int cb) {
  const int tid = threadIdx.x, lane = tid & 63;
  const int w = __builtin_amdgcn_readfirstlane(tid >> 6);
  const float* modp = (const float*)(ws + WS_MODP);
  float* sc = (float*)(smem + 33792 + 32768);
  const unsigned l16 = lane * 16;
  u32x4 xr[4];
  {
    const char* xp = (const char*)(x + (size_t)(rt * 32 + w * 4) * 512);
    GLD(xr[0], l16, xp, 0); GLD(xr[1], l16, xp, 1024); GLD(xr[2], l16, xp, 2048); GLD(xr[3], l16, xp, 3072);
  }
  float mp[8];
#pragma unroll
  for (int i = 0; i < 4; ++i) { GLD1(mp[i], tid * 4, modp + i * 3072, 0); GLD1(mp[4 + i], tid * 4, modp + i * 3072, 2048); }
  u32x4 wf[16];
  {
    const char* p = iskv ? ws + WS_WKVF + w * 4096 : ws + WS_WQF + (size_t)((cb * 4 + (w & 3)) * 32 + (w >> 2) * 16) * 1024;
    GLD(wf[0], l16, p, 0); GLD(wf[1], l16, p, 1024); GLD(wf[2], l16, p, 2048); GLD(wf[3], l16, p, 3072);
    if (!iskv) {
#pragma unroll
      for (int i = 1; i < 4; ++i) {
        const char* q = p + i * 4096;
        GLD(wf[4 * i], l16, q, 0); GLD(wf[4 * i + 1], l16, q, 1024); GLD(wf[4 * i + 2], l16, q, 2048); GLD(wf[4 * i + 3], l16, q, 3072);
      }
      asm volatile("s_waitcnt vmcnt(16)" : "+v"(xr[0]), "+v"(xr[1]), "+v"(xr[2]), "+v"(xr[3]), "+v"(mp[0]), "+v"(mp[1]), "+v"(mp[2]),
                   "+v"(mp[3]), "+v"(mp[4]), "+v"(mp[5]), "+v"(mp[6]), "+v"(mp[7]));
    } else {
      asm volatile("s_waitcnt vmcnt(4)" : "+v"(xr[0]), "+v"(xr[1]), "+v"(xr[2]), "+v"(xr[3]), "+v"(mp[0]), "+v"(mp[1]), "+v"(mp[2]),
                   "+v"(mp[3]), "+v"(mp[4]), "+v"(mp[5]), "+v"(mp[6]), "+v"(mp[7]));
    }
  }
  TS(1, 1);
  sc[tid] = (mp[0] + mp[1]) + (mp[2] + mp[3]);
  sc[512 + tid] = 1.f + ((mp[4] + mp[5]) + (mp[6] + mp[7]));
  float v[4][8];
#pragma unroll
  for (int i = 0; i < 4; ++i) {
#pragma unroll
    for (int j = 0; j < 4; ++j) { v[i][2 * j] = bflo(xr[i][j]); v[i][2 * j + 1] = bfhi(xr[i][j]); }
    float s = ((v[i][0] + v[i][1]) + (v[i][2] + v[i][3])) + ((v[i][4] + v[i][5]) + (v[i][6] + v[i][7]));
    const float mean = wave_sum(s) * (1.f / 512.f);
    float q = 0.f;
#pragma unroll
    for (int j = 0; j < 8; ++j) { v[i][j] -= mean; q += v[i][j] * v[i][j]; }
    const float r = 1.f / sqrtf(wave_sum(q) * (1.f / 512.f) + 1e-5f);
#pragma unroll
    for (int j = 0; j < 8; ++j) v[i][j] *= r;
  }
  asm volatile("s_waitcnt lgkmcnt(0)\n\ts_barrier" ::: "memory");
  {
    const f32x4 s0 = *(const f32x4*)(sc + lane * 8), s1 = *(const f32x4*)(sc + lane * 8 + 4);
    const f32x4 c0 = *(const f32x4*)(sc + 512 + lane * 8), c1 = *(const f32x4*)(sc + 512 + lane * 8 + 4);
#pragma unroll
    for (int i = 0; i < 4; ++i) {
      u32x4 o;
      o[0] = pack2(v[i][0] * c0[0] + s0[0], v[i][1] * c0[1] + s0[1]);
      o[1] = pack2(v[i][2] * c0[2] + s0[2], v[i][3] * c0[3] + s0[3]);
      o[2] = pack2(v[i][4] * c1[0] + s1[0], v[i][5] * c1[1] + s1[1]);
      o[3] = pack2(v[i][6] * c1[2] + s1[2], v[i][7] * c1[3] + s1[3]);
      *(u32x4*)(smem + lane * 528 + (w * 4 + i) * 16) = o;
    }
  }
  asm volatile("s_waitcnt vmcnt(0)" : "+v"(wf[0]), "+v"(wf[1]), "+v"(wf[2]), "+v"(wf[3]));
  if (!iskv)
    asm volatile("" : "+v"(wf[4]), "+v"(wf[5]), "+v"(wf[6]), "+v"(wf[7]), "+v"(wf[8]), "+v"(wf[9]), "+v"(wf[10]), "+v"(wf[11]),
                 "+v"(wf[12]), "+v"(wf[13]), "+v"(wf[14]), "+v"(wf[15]));
  if (iskv && rt < 6) {
    const int i = rt * 512 + tid;
    ((float*)(ws + WS_MODF))[i] = (modp[i] + modp[3072 + i]) + (modp[6144 + i] + modp[9216 + i]);
  }
  __syncthreads();
  TS(1, 2);
  const char* hb = smem + (lane >> 5) * 528 + (lane & 31) * 16;
  char* red = smem + 33792;
  f32x16 acc = zero16();
  if (iskv) {
#pragma unroll
    for (int i = 0; i < 4; ++i) acc = mfma(wf[i], *(const u32x4*)(hb + (w * 4 + i) * 1056), acc);
#pragma unroll
    for (int q = 0; q < 4; ++q)
      *(f32x4*)(red + (w * 4 + q) * 1024 + lane * 16) = f32x4{acc[4 * q], acc[4 * q + 1], acc[4 * q + 2], acc[4 * q + 3]};
    __syncthreads();
    if (tid < 256) {
      const int q = w;
      f32x4 s = {0, 0, 0, 0};
#pragma unroll
      for (int p = 0; p < 8; ++p) s += *(const f32x4*)(red + (p * 4 + q) * 1024 + lane * 16);
      const unsigned p0 = pack2(s[0], s[1]), p1 = pack2(s[2], s[3]);
      if (q < 2) {
        *(u32x2*)(ws + WS_KF + rt * 1024 + lane * 16 + q * 8) = u32x2{p0, p1};
      } else {
        const int t = lane & 31, h = lane >> 5;
        const int d0 = 8 * (q - 2) + 4 * h;
        u16* vp = (u16*)(ws + WS_VF + rt * 1024 + (t >> 4) * 512 + ((t >> 2) & 1) * 256 + d0 * 16) + (((t >> 3) & 1) * 4 + (t & 3));
        vp[0] = (u16)p0; vp[8] = (u16)(p0 >> 16); vp[16] = (u16)p1; vp[24] = (u16)(p1 >> 16);
      }
    }
  } else {
    const int kh = w >> 2, ct = w & 3;
#pragma unroll
    for (int i = 0; i < 16; ++i) acc = mfma(wf[i], *(const u32x4*)(hb + (kh * 16 + i) * 1056), acc);
    f32x4 k0, k1, g0, g1;
    if (kh == 0) {
      k0 = f32x4{acc[0], acc[1], acc[2], acc[3]}; k1 = f32x4{acc[4], acc[5], acc[6], acc[7]};
      g0 = f32x4{acc[8], acc[9], acc[10], acc[11]}; g1 = f32x4{acc[12], acc[13], acc[14], acc[15]};
    } else {
      g0 = f32x4{acc[0], acc[1], acc[2], acc[3]}; g1 = f32x4{acc[4], acc[5], acc[6], acc[7]};
      k0 = f32x4{acc[8], acc[9], acc[10], acc[11]}; k1 = f32x4{acc[12], acc[13], acc[14], acc[15]};
    }
    *(f32x4*)(red + w * 2048 + lane * 16) = g0;
    *(f32x4*)(red + w * 2048 + 1024 + lane * 16) = g1;
    __syncthreads();
    k0 += *(const f32x4*)(red + (w ^ 4) * 2048 + lane * 16);
    k1 += *(const f32x4*)(red + (w ^ 4) * 2048 + 1024 + lane * 16);
    const float sc = 0.25f * 1.4426950408889634f;
    k0 *= sc; k1 *= sc;
    u32x4 o = {pack2(k0[0], k0[1]), pack2(k0[2], k0[3]), pack2(k1[0], k1[1]), pack2(k1[2], k1[3])};
    const int head = (cb * 4 + ct) * 2 + kh;
    *(u32x4*)(ws + WS_QF + ((size_t)(rt * 32 + head) * 64 + lane) * 16) = o;
  }
}

__global__ __launch_bounds__(512, 4) void k2_qkv(const u16* __restrict__ x, const u16* __restrict__ wg, const u16* __restrict__ wu,
                                                 char* __restrict__ ws) {
  __shared__ __attribute__((aligned(16))) char smem[33792 + 32768 + 4096];
  const int tid = threadIdx.x, lane = tid & 63;
  const int w = __builtin_amdgcn_readfirstlane(tid >> 6);
  const float* modp = (const float*)(ws + WS_MODP);
  float* sc = (float*)(smem + 33792 + 32768);
  int b = blockIdx.x;
  TS(1, 0);
  if (b < 56) {
    const int up = b >= 28, f0 = (b % 28) * 64;
    const int c8 = tid & 7, kg = tid >> 3;
    const u16* src = (up ? wu : wg) + (size_t)(kg * 8) * 1792 + f0 + c8 * 8;
    u32x4 wr[8];
#pragma unroll
    for (int i = 0; i < 8; ++i) wr[i] = *(const u32x4*)(src + i * 1792);
    {
      const float* mp = modp + 1536 + tid;
      sc[tid] = (mp[0] + mp[3072]) + (mp[6144] + mp[9216]);
      sc[512 + tid] = 1.f + ((mp[512] + mp[3584]) + (mp[6656] + mp[9728]));
    }
    __syncthreads();
    const f32x4 s0 = *(const f32x4*)(sc + kg * 8), s1 = *(const f32x4*)(sc + kg * 8 + 4);
    const f32x4 c0 = *(const f32x4*)(sc + 512 + kg * 8), c1 = *(const f32x4*)(sc + 512 + kg * 8 + 4);
    float au[8], ab[8];
#pragma unroll
    for (int j = 0; j < 8; ++j) au[j] = ab[j] = 0.f;
#pragma unroll
    for (int i = 0; i < 8; ++i) {
      const float c = i < 4 ? c0[i & 3] : c1[i & 3], s = i < 4 ? s0[i & 3] : s1[i & 3];
#pragma unroll
      for (int j = 0; j < 4; ++j) {
        const float lo = bflo(wr[i][j]), hi = bfhi(wr[i][j]);
        au[2 * j] += c * lo; au[2 * j + 1] += c * hi; ab[2 * j] += s * lo; ab[2 * j + 1] += s * hi;
      }
    }
#pragma unroll
    for (int j = 0; j < 8; ++j) { au[j] += dppf<0x128>(au[j]); ab[j] += dppf<0x128>(ab[j]); }
    float* part = (float*)smem;
    if ((lane & 15) < 8) {
      float* p = part + (w * 4 + (lane >> 4)) * 128 + c8 * 16;
      *(f32x4*)p = f32x4{au[0], au[1], au[2], au[3]};
      *(f32x4*)(p + 4) = f32x4{au[4], au[5], au[6], au[7]};
      *(f32x4*)(p + 8) = f32x4{ab[0], ab[1], ab[2], ab[3]};
      *(f32x4*)(p + 12) = f32x4{ab[4], ab[5], ab[6], ab[7]};
    }
    __syncthreads();
    const int col = tid >> 3, seg = tid & 7;
    float u = 0.f, bb = 0.f;
#pragma unroll
    for (int i = 0; i < 4; ++i) {
      const float* p = part + (seg * 4 + i) * 128 + (col >> 3) * 16 + (col & 7);
      u += p[0]; bb += p[8];
    }
    u = red8(u); bb = red8(bb);
    if (seg < 2) {
      unsigned uh, ul, bh, bl;
      split_bf(u, uh, ul); split_bf(bb, bh, bl);
      u32x4 o = {uh | (ul << 16), uh | (ul << 16), bh | (bl << 16), bh | (bl << 16)};
      if (seg) o = u32x4{0, 0, 0, 0};
      const int f = f0 + col, t = f >> 4, m = (f & 15) + 16 * up + 32 * seg;
      *(u32x4*)(ws + WS_WGUF + ((size_t)(t * 33 + 32) * 64 + m) * 16) = o;
    }
    TS(1, 7);
    return;
  }
  b -= 56;
  if (b < 64) qkv_body<true>(x, ws, smem, b, 0);
  else qkv_body<false>(x, ws, smem, b & 63, (b >> 6) - 1);
  TS(1, 6);
}

DEV float xhalf_max(float m) { return fmaxf(m, __shfl_xor(m, 32)); }
DEV float max16(const f32x16& s) {
  float a = fmaxf(fmaxf(s[0], s[1]), s[2]), b = fmaxf(fmaxf(s[3], s[4]), s[5]), c = fmaxf(fmaxf(s[6], s[7]), s[8]);
  float d = fmaxf(fmaxf(s[9], s[10]), s[11]), e = fmaxf(fmaxf(s[12], s[13]), s[14]);
  return fmaxf(fmaxf(fmaxf(a, b), c), fmaxf(fmaxf(d, e), s[15]));
}
#define ATT_LOOP \
  "1:\n\tv_exp_f32 v0, v0\n\tv_exp_f32 v1, v1\n\tv_exp_f32 v2, v2\n\t" \
  "v_exp_f32 v3, v3\n\tv_mfma_f32_32x32x16_bf16 v[48:63], v[88:91], v[96:99], v[48:63]\n\ts_waitcnt lgkmcnt(0)\n\tds_read_b128 v[68:71], v100 offset:2048\n\t" \
  "s_mov_b64 exec, %[msk]\n\tds_read_b128 v[84:87], v101 offset:1024\n\tds_read_b128 v[88:91], v101 offset:1536\n\ts_mov_b64 exec, -1\n\t" \
  "v_cvt_pk_bf16_f32 v92, v0, v1\n\tv_exp_f32 v4, v4\n\tv_exp_f32 v5, v5\n\tv_mfma_f32_32x32x16_bf16 v[16:31], v[72:75], v[64:67], v[32:47]\n\t" \
  "v_cvt_pk_bf16_f32 v93, v2, v3\n\tv_exp_f32 v6, v6\n\tv_exp_f32 v7, v7\n\tv_cvt_pk_bf16_f32 v94, v4, v5\n\t" \
  "v_exp_f32 v8, v8\n\tv_exp_f32 v9, v9\n\tv_cvt_pk_bf16_f32 v95, v6, v7\n\tv_exp_f32 v10, v10\n\t" \
  "v_exp_f32 v11, v11\n\tv_mfma_f32_32x32x16_bf16 v[48:63], v[76:79], v[92:95], v[48:63]\n\tv_cvt_pk_bf16_f32 v96, v8, v9\n\tv_exp_f32 v12, v12\n\t" \
  "v_exp_f32 v13, v13\n\tv_cvt_pk_bf16_f32 v97, v10, v11\n\tv_exp_f32 v14, v14\n\tv_exp_f32 v15, v15\n\t" \
  "v_cvt_pk_bf16_f32 v98, v12, v13\n\tv_cvt_pk_bf16_f32 v99, v14, v15\n\tv_exp_f32 v16, v16\n\tv_exp_f32 v17, v17\n\t" \
  "v_exp_f32 v18, v18\n\tv_exp_f32 v19, v19\n\tv_mfma_f32_32x32x16_bf16 v[48:63], v[80:83], v[96:99], v[48:63]\n\ts_waitcnt lgkmcnt(0)\n\t" \
  "ds_read_b128 v[72:75], v100 offset:3072\n\ts_mov_b64 exec, %[msk]\n\tds_read_b128 v[76:79], v101 offset:2048\n\tds_read_b128 v[80:83], v101 offset:2560\n\t" \
  "s_mov_b64 exec, -1\n\tv_cvt_pk_bf16_f32 v92, v16, v17\n\tv_exp_f32 v20, v20\n\tv_exp_f32 v21, v21\n\t" \
  "v_mfma_f32_32x32x16_bf16 v[0:15], v[68:71], v[64:67], v[32:47]\n\tv_cvt_pk_bf16_f32 v93, v18, v19\n\tv_exp_f32 v22, v22\n\tv_exp_f32 v23, v23\n\t" \
  "v_cvt_pk_bf16_f32 v94, v20, v21\n\tv_exp_f32 v24, v24\n\tv_exp_f32 v25, v25\n\tv_cvt_pk_bf16_f32 v95, v22, v23\n\t" \
  "v_exp_f32 v26, v26\n\tv_exp_f32 v27, v27\n\tv_mfma_f32_32x32x16_bf16 v[48:63], v[84:87], v[92:95], v[48:63]\n\tv_add_u32 v100, 0x800, v100\n\t" \
  "v_add_u32 v101, 0x800, v101\n\ts_sub_u32 %[n], %[n], 1\n\ts_cmp_lg_u32 %[n], 0\n\tv_cvt_pk_bf16_f32 v96, v24, v25\n\t" \
  "v_exp_f32 v28, v28\n\tv_exp_f32 v29, v29\n\tv_cvt_pk_bf16_f32 v97, v26, v27\n\tv_exp_f32 v30, v30\n\t" \
  "v_exp_f32 v31, v31\n\tv_cvt_pk_bf16_f32 v98, v28, v29\n\tv_cvt_pk_bf16_f32 v99, v30, v31\n\ts_cbranch_scc1 1b\n\t" \
  "s_nop 1\n\tv_mfma_f32_32x32x16_bf16 v[48:63], v[88:91], v[96:99], v[48:63]\n\ts_nop 7\n\tv_mov_b32 v96, 0\n\t" \
  "v_mov_b32 v97, 0\n\tv_mov_b32 v98, 0\n\tv_mov_b32 v99, 0\n\ts_waitcnt lgkmcnt(0)\n\t" \
  "s_nop 15\n\t\n\t" \
  ""
#define LDK(dst, addr, off) asm volatile("ds_read_b128 %0, %1 offset:" #off : "=v"(dst) : "v"(addr))
#define LDV(d0, d1, addr, off0, off1)                                                                                   \
  asm volatile("s_mov_b64 exec, %3\n\tds_read_b128 %0, %2 offset:" #off0 "\n\tds_read_b128 %1, %2 offset:" #off1        \
               "\n\ts_mov_b64 exec, -1"                                                                                 \
               : "+v"(d0), "+v"(d1)                                                                                     \
               : "v"(addr), "s"(vmask))
#define LWAIT(a, b, c) asm volatile("s_waitcnt lgkmcnt(0)" : "+v"(a), "+v"(b), "+v"(c))

#define LDH(d0, d1, p) \
  asm volatile("global_load_ushort %0, %2, off\n\tglobal_load_ushort %1, %2, off offset:64" : "=&v"(d0), "=&v"(d1) : "v"(p) : "memory")
__global__ __launch_bounds__(1024, 4) void k3_attn(const u16* __restrict__ wo, const u16* __restrict__ wg, const u16* __restrict__ wu,
                                                   const u16* __restrict__ wd, char* __restrict__ ws) {
  __shared__ __attribute__((aligned(16))) char smem[131072 + 4096];
  const int tid = threadIdx.x, lane = tid & 63;
  const int w = __builtin_amdgcn_readfirstlane(tid >> 6);
  const int rt = blockIdx.x & 63, hg = blockIdx.x >> 6;
  TS(2, 0);
  const int kh = w >> 3;
  const unsigned sbase = (unsigned)(uintptr_t)smem;
  const int head = hg * 8 + (w & 7);
  u32x4 q;
  {
    const unsigned l16 = lane * 16;
    GLD(q, l16, ws + WS_QF + (size_t)(rt * 32 + head) * 1024, 0);
    const char* kp = ws + WS_KF + kh * 32768 + (w & 7) * 1024;
    const unsigned lb = sbase + kh * 32768 + (w & 7) * 1024;
#pragma unroll
    for (int i = 0; i < 4; ++i) {
      dma16(l16, kp + i * 8192, lb + i * 8192);
      dma16(l16, kp + i * 8192 + 65536, lb + i * 8192 + 65536);
    }
  }
  unsigned f0[8], f1[8];
  char* fdst;
  unsigned fstep;
  bool fstore;
  {
    int p = blockIdx.x * 12 + (w < 12 ? w : w - 4);
    fstore = w < 12 && p < 2944;
    if (p >= 2944) p -= 128;
    const int h = lane >> 5, m = lane & 31;
    const u16* src;
    int ld, ka, kb2;
    if (p < 1792) {
      const int t = (p >> 6) * 4 + (p & 1), ks = (p >> 1) & 31;
      ld = 1792; ka = 8 * h; kb2 = ka + 4;
      src = (m < 16 ? wg : wu) + ks * 16 * 1792 + t * 16 + (m & 15);
      fdst = ws + WS_WGUF + (size_t)(t * 33 + ks) * 1024; fstep = 2 * 33 * 1024;
    } else {
      const bool isd = p < 2688;
      const int pp = p - (isd ? 1792 : 2688), t = (pp & 7) * 2, ks = pp >> 3, KS = isd ? 112 : 32;
      ld = 512; ka = 4 * h; kb2 = ka + 8;
      src = (isd ? wd : wo) + ks * 16 * 512 + t * 32 + m;
      fdst = ws + (isd ? WS_WDF : WS_WOF) + (size_t)(t * KS + ks) * 1024; fstep = KS * 1024;
    }
    fdst += lane * 16;
    const u16 *pa = src + ka * ld, *pb = src + kb2 * ld;
    const u16* rp[8] = {pa, pa + ld, pa + 2 * ld, pa + 3 * ld, pb, pb + ld, pb + 2 * ld, pb + 3 * ld};
    asm volatile("s_waitcnt vmcnt(6)\n\ts_barrier" : "+v"(q)::"memory");
#pragma unroll
    for (int j = 0; j < 8; ++j) LDH(f0[j], f1[j], rp[j]);
  }
  const unsigned long long vmask = 0x0000ffff0000ffffull;
  const unsigned kbase = sbase + kh * 32768 + lane * 16;
  const unsigned vbase = sbase + 65536 + kh * 32768 + (lane >> 5) * 256 + (lane & 15) * 16;
  const unsigned one1 = (lane & 27) == 16 ? 0x3f803f80u : 0u;
  const u32x4 ones = {one1, one1, one1, one1};
  u32x4 va0 = ones, va1 = ones, vb0 = ones, vb1 = ones, ka, kb;
  TS(2, 1);
  f32x16 o;
  float off;
  for (int attempt = 0; attempt < 2; ++attempt) {
    float m = -3.0e38f;
    const int ntm = attempt ? 32 : 1;
    for (int t = 0; t < ntm; ++t) {
      const unsigned a = kbase + t * 1024;
      LDK(ka, a, 0);
      LWAIT(ka, va0, va1);
      m = fmaxf(m, max16(mfma(ka, q, zero16())));
    }
    m = xhalf_max(m);
    const float thr = attempt ? __builtin_inff() : 1.f;
    off = m + (attempt ? 0.f : 80.f);
    f32x16 c;
#pragma unroll
    for (int i = 0; i < 16; ++i) c[i] = -off;
    o = zero16();
    unsigned kad = kbase, vad = vbase;
    LDK(ka, kad, 0);
    LDK(kb, kad, 1024);
    LDV(va0, va1, vad, 0, 512);
    asm volatile("s_waitcnt lgkmcnt(0)" : "+v"(ka), "+v"(kb), "+v"(va0), "+v"(va1));
    f32x16 s0 = mfma(ka, q, c);
    u32x4 pz = {0, 0, 0, 0};
    for (int seg = 0; seg < 3; ++seg) {
      if (seg == 1 && attempt == 0) asm volatile("s_waitcnt vmcnt(16)\n\ts_barrier" ::: "memory");
      if (seg == 2 && attempt == 0) {
        asm volatile("s_waitcnt vmcnt(0)"
                     : "+v"(f0[0]), "+v"(f0[1]), "+v"(f0[2]), "+v"(f0[3]), "+v"(f0[4]), "+v"(f0[5]), "+v"(f0[6]), "+v"(f0[7]),
                       "+v"(f1[0]), "+v"(f1[1]), "+v"(f1[2]), "+v"(f1[3]), "+v"(f1[4]), "+v"(f1[5]), "+v"(f1[6]), "+v"(f1[7])::"memory");
        if (fstore) {
          const u32x4 g0 = {f0[0] | (f0[1] << 16), f0[2] | (f0[3] << 16), f0[4] | (f0[5] << 16), f0[6] | (f0[7] << 16)};
          const u32x4 g1 = {f1[0] | (f1[1] << 16), f1[2] | (f1[3] << 16), f1[4] | (f1[5] << 16), f1[6] | (f1[7] << 16)};
          asm volatile("global_store_dwordx4 %0, %1, off sc1\n\tglobal_store_dwordx4 %2, %3, off sc1\n\ts_nop 1"
                       ::"v"(fdst), "v"(g0), "v"(fdst + fstep), "v"(g1) : "memory");
        }
      }
      if (seg) {
        if (__any(o[8] > thr)) {
          const int e = __builtin_amdgcn_frexp_expf(o[8]) + 80;
          const float f = (float)e;
#pragma unroll
          for (int i = 0; i < 9; ++i) o[i] = __builtin_amdgcn_ldexpf(o[i], -e);
          off += f;
#pragma unroll
          for (int i = 0; i < 16; ++i) { c[i] = -off; s0[i] -= f; }
        }
      }
      int np = seg == 0 ? 2 : seg == 1 ? 5 : 9;
      asm volatile("s_nop 15\n\t" ATT_LOOP
                   : "+{v[0:15]}"(s0), "+{v[48:63]}"(o), "+{v[72:75]}"(kb), "+{v[76:79]}"(va0), "+{v[80:83]}"(va1),
                     "+{v[84:87]}"(vb0), "+{v[88:91]}"(vb1), "+{v[96:99]}"(pz), "+{v100}"(kad), "+{v101}"(vad), [n] "+s"(np)
                   : "{v[32:47]}"(c), "{v[64:67]}"(q), [msk] "s"(vmask)
                   : "v16", "v17", "v18", "v19", "v20", "v21", "v22", "v23", "v24", "v25", "v26", "v27", "v28", "v29", "v30", "v31",
                     "v68", "v69", "v70", "v71", "v92", "v93", "v94", "v95", "scc", "memory");
    }
    if (attempt) break;
    float a = 0.f, s = o[8];
#pragma unroll
    for (int i = 0; i < 8; ++i) { a = fmaxf(a, fabsf(o[i])); s += o[i] * 0.f; }
    const bool ok = (s < 3.0e38f) && (a >= 7.8886091e-31f);
    if (!__any(!ok)) break;
  }
  TS(2, 2);
  __syncthreads();
  char* ex = smem + (w & 7) * 3072 + lane * 16;
  if (kh) {
    *(f32x4*)(ex) = f32x4{o[0], o[1], o[2], o[3]};
    *(f32x4*)(ex + 1024) = f32x4{o[4], o[5], o[6], o[7]};
    *(f32x2*)(ex + 2048) = f32x2{o[8], off};
  }
  __syncthreads();
  if (!kh) {
    const f32x4 g0 = *(const f32x4*)(ex), g1 = *(const f32x4*)(ex + 1024);
    const f32x2 g2 = *(const f32x2*)(ex + 2048);
    const float m = fmaxf(off, g2[1]);
    float fk = __builtin_amdgcn_exp2f(off - m), fg = __builtin_amdgcn_exp2f(g2[1] - m);
    const float inv = 1.f / (o[8] * fk + g2[0] * fg);
    fk *= inv; fg *= inv;
    const f32x4 k0 = f32x4{o[0], o[1], o[2], o[3]} * fk + g0 * fg, k1 = f32x4{o[4], o[5], o[6], o[7]} * fk + g1 * fg;
    const u32x4 ov = {pack2(k0[0], k0[1]), pack2(k0[2], k0[3]), pack2(k1[0], k1[1]), pack2(k1[2], k1[3])};
    *(u32x4*)(ws + WS_OF + ((size_t)(rt * 32 + head) * 64 + lane) * 16) = ov;
  }
  TS(2, 6);
}

template <int KS, int DEPTH, bool FINAL>
__global__ __launch_bounds__(256, 1) void k46_gemm(const u16* __restrict__ res, const char* __restrict__ af, const char* __restrict__ wf,
                                                   char* __restrict__ ws, u16* __restrict__ out) {
  __shared__ __attribute__((aligned(16))) float red[4 * 64 * 68];
  const int tid = threadIdx.x, lane = tid & 63;
  const int w = __builtin_amdgcn_readfirstlane(tid >> 6);
  const int b = blockIdx.x;
  const int rb = (b & 7) * 4 + ((b >> 3) & 3), cbk = b >> 5;
  constexpr int N = KS / 4;
  TS(FINAL ? 5 : 3, 0);
  const unsigned l16 = lane * 16;
  const char* ap = af + (size_t)(rb * 2 * KS + w * N) * 1024;
  const char* wp = wf + (size_t)(cbk * 2 * KS + w * N) * 1024;
  u32x4 fa[DEPTH][2], fw[DEPTH][2];
#define LD4(s, k)                                                                                     \
  {                                                                                                   \
    GLD(fa[s][0], l16, ap + ((k) >> 2) * 4096, ((k) & 3) * 1024);                                     \
    GLD(fw[s][0], l16, wp + ((k) >> 2) * 4096, ((k) & 3) * 1024);                                     \
    GLD(fa[s][1], l16, ap + KS * 1024 + ((k) >> 2) * 4096, ((k) & 3) * 1024);                         \
    GLD(fw[s][1], l16, wp + KS * 1024 + ((k) >> 2) * 4096, ((k) & 3) * 1024);                         \
  }
#pragma unroll
  for (int i = 0; i < DEPTH; ++i) LD4(i, i)
  const int cg = tid & 7, r0 = tid >> 3;
  const int col = cbk * 64 + cg * 8;
  const float* modf = (const float*)(ws + WS_MODF);
  u32x4 xr[2], g0u, g1u, c0u, c1u;
  {
    const char* rp = (const char*)(res + (size_t)(rb * 64) * 512 + cbk * 64);
    const unsigned ro = r0 * 1024 + cg * 16;
    GLD(xr[0], ro, rp, 0); GLD(xr[1], ro, rp + 32768, 0);
    const unsigned mo = cg * 32;
    const char* gp = (const char*)(modf + (FINAL ? 2560 : 1024) + cbk * 64);
    GLD(g0u, mo, gp, 0); GLD(g1u, mo, gp, 16);
    const char* cp = (const char*)(modf + 2048 + cbk * 64);
    GLD(c0u, mo, cp, 0); GLD(c1u, mo, cp, 16);
  }
  f32x16 acc[2][2];
  acc[0][0] = acc[0][1] = acc[1][0] = acc[1][1] = zero16();
#pragma unroll
  for (int k = 0; k < N; ++k) {
    const int s = k % DEPTH;
    const int after = 4 * ((k + DEPTH < N ? k + DEPTH : N) - k - 1) + (k < DEPTH ? 6 : 0);
    asm volatile("s_waitcnt vmcnt(%4)" : "+v"(fa[s][0]), "+v"(fa[s][1]), "+v"(fw[s][0]), "+v"(fw[s][1]) : "n"(after));
    if (k == 0) TS(FINAL ? 5 : 3, 1);
    acc[0][0] = mfma(fw[s][0], fa[s][0], acc[0][0]);
    acc[0][1] = mfma(fw[s][0], fa[s][1], acc[0][1]);
    acc[1][0] = mfma(fw[s][1], fa[s][0], acc[1][0]);
    acc[1][1] = mfma(fw[s][1], fa[s][1], acc[1][1]);
    if (k + DEPTH < N) LD4(s, k + DEPTH)
  }
  asm volatile("s_waitcnt vmcnt(0)" : "+v"(xr[0]), "+v"(xr[1]), "+v"(g0u), "+v"(g1u), "+v"(c0u), "+v"(c1u));
  TS(FINAL ? 5 : 3, 2);
  const f32x4 g0 = __builtin_bit_cast(f32x4, g0u), g1 = __builtin_bit_cast(f32x4, g1u);
  const f32x4 c0 = __builtin_bit_cast(f32x4, c0u) + 1.f, c1 = __builtin_bit_cast(f32x4, c1u) + 1.f;
  {
    float* rp = red + w * (64 * 68) + (lane & 31) * 68 + (lane >> 5) * 4;
#pragma unroll
    for (int i = 0; i < 2; ++i)
#pragma unroll
      for (int j = 0; j < 2; ++j)
#pragma unroll
        for (int q = 0; q < 4; ++q)
          *(f32x4*)(rp + j * 32 * 68 + i * 32 + q * 8) =
              f32x4{acc[i][j][4 * q], acc[i][j][4 * q + 1], acc[i][j][4 * q + 2], acc[i][j][4 * q + 3]};
  }
  __syncthreads();
#pragma unroll
  for (int e = 0; e < 2; ++e) {
    const int r = r0 + 32 * e, row = rb * 64 + r;
    f32x4 s0 = {0, 0, 0, 0}, s1 = s0;
#pragma unroll
    for (int p = 0; p < 4; ++p) {
      const float* rp = red + p * (64 * 68) + r * 68 + cg * 8;
      s0 += *(const f32x4*)rp; s1 += *(const f32x4*)(rp + 4);
    }
    const f32x4 x0 = {bflo(xr[e][0]), bfhi(xr[e][0]), bflo(xr[e][1]), bfhi(xr[e][1])};
    const f32x4 x1 = {bflo(xr[e][2]), bfhi(xr[e][2]), bflo(xr[e][3]), bfhi(xr[e][3])};
    s0 = x0 + g0 * s0; s1 = x1 + g1 * s1;
    const u32x4 o = {pack2(s0[0], s0[1]), pack2(s0[2], s0[3]), pack2(s1[0], s1[1]), pack2(s1[2], s1[3])};
    *(u32x4*)(out + (size_t)row * 512 + col) = o;
    if (!FINAL) {
      const f32x4 y0 = s0 * c0, y1 = s1 * c1;
      const u32x4 y = {pack2(y0[0], y0[1]), pack2(y0[2], y0[3]), pack2(y1[0], y1[1]), pack2(y1[2], y1[3])};
      *(u32x4*)(ws + WS_YF + (size_t)((row >> 5) * 32 + (col >> 4)) * 1024 + ((col >> 3) & 1) * 512 + (row & 31) * 16) = y;
      const float mean = red8(((s0[0] + s0[1]) + (s0[2] + s0[3])) + ((s1[0] + s1[1]) + (s1[2] + s1[3]))) * (1.f / 64.f);
      s0 -= mean; s1 -= mean;
      s0 *= s0; s1 *= s1;
      const float m2 = red8(((s0[0] + s0[1]) + (s0[2] + s0[3])) + ((s1[0] + s1[1]) + (s1[2] + s1[3])));
      if (cg == 0) *(f32x2*)(ws + WS_STAT + (size_t)(row * 8 + cbk) * 8) = f32x2{mean, m2};
    }
  }
  TS(FINAL ? 5 : 3, 6);
}

__global__ __launch_bounds__(256, 1) void k5_ffn(char* __restrict__ ws) {
  __shared__ __attribute__((aligned(16))) char smem[5 * 28672 + 7168];
  const int tid = threadIdx.x, lane = tid & 63;
  const int w = __builtin_amdgcn_readfirstlane(tid >> 6);
  const int b = blockIdx.x, xc = b & 7, idx = b >> 3;
  const int rb = (xc & 1) * 8 + (idx & 7), cbk = (xc >> 1) * 4 + (idx >> 3);
  const int rt = rb * 4 + w, t0 = cbk * 7;
  TS(4, 0);
  const unsigned sbase = (unsigned)(uintptr_t)smem;
  const unsigned l16 = lane * 16;
  const char* wsrc = ws + WS_WGUF + (size_t)t0 * 33 * 1024;
  const char* ysrc = ws + WS_YF + (size_t)rt * 32 * 1024;
  u32x4 st[4], y[32];
  {
    const char* sp = ws + WS_STAT + (size_t)(rt * 32) * 64;
    const unsigned so = (lane & 31) * 64;
    GLD(st[0], so, sp, 0); GLD(st[1], so, sp, 16); GLD(st[2], so, sp, 32); GLD(st[3], so, sp, 48);
    const int i2 = w + 4 < 7 ? w + 4 : 6;
    dma16(l16, wsrc + (w * 33 + 32) * 1024, sbase + 5 * 28672 + w * 1024);
    dma16(l16, wsrc + (i2 * 33 + 32) * 1024, sbase + 5 * 28672 + i2 * 1024);
  }
#define ISSUE(c)                                                                                             \
  {                                                                                                          \
    _Pragma("unroll") for (int i = 0; i < 7; ++i)                                                            \
        dma16(l16, wsrc + (i * 33 + 4 * (c) + w) * 1024, sbase + ((c) % 5) * 28672 + (w * 7 + i) * 1024);    \
    const char* yp = ysrc + (c) * 4096;                                                                      \
    GLD(y[4 * (c)], l16, yp, 0); GLD(y[4 * (c) + 1], l16, yp, 1024);                                         \
    GLD(y[4 * (c) + 2], l16, yp, 2048); GLD(y[4 * (c) + 3], l16, yp, 3072);                                  \
  }
#define WAITB(n, c)                                                                                          \
  asm volatile("s_waitcnt vmcnt(" #n ")\n\ts_barrier"                                                        \
               : "+v"(y[4 * (c)]), "+v"(y[4 * (c) + 1]), "+v"(y[4 * (c) + 2]), "+v"(y[4 * (c) + 3])::"memory")
  ISSUE(0) ISSUE(1) ISSUE(2) ISSUE(3) ISSUE(4)
  f32x16 acc[7];
#pragma unroll
  for (int i = 0; i < 7; ++i) acc[i] = zero16();
  const char* lp = smem + lane * 16;
  u32x4 wc[7], wn[7];
  WAITB(44, 0);
  asm volatile("" : "+v"(st[0]), "+v"(st[1]), "+v"(st[2]), "+v"(st[3]));
#pragma unroll
  for (int i = 0; i < 7; ++i) wc[i] = *(const u32x4*)(lp + i * 1024);
  TS(4, 1);
  float r2;
  u32x4 yx;
  {
    float mb[8], m2 = 0.f, mean = 0.f;
#pragma unroll
    for (int i = 0; i < 4; ++i) {
      mb[2 * i] = __uint_as_float(st[i][0]); mb[2 * i + 1] = __uint_as_float(st[i][2]);
      m2 += __uint_as_float(st[i][1]) + __uint_as_float(st[i][3]);
      mean += mb[2 * i] + mb[2 * i + 1];
    }
    mean *= 0.125f;
    float d = 0.f;
#pragma unroll
    for (int i = 0; i < 8; ++i) d += (mb[i] - mean) * (mb[i] - mean);
    const float var = (m2 + 64.f * d) * (1.f / 512.f) + 1e-5f;
    r2 = 1.f / var;
    unsigned mh, ml, rh, rlo;
    split_bf(-mean, mh, ml); split_bf(sqrtf(var), rh, rlo);
    yx = u32x4{mh | (mh << 16), ml | (ml << 16), rh | (rh << 16), rlo | (rlo << 16)};
    if (lane >= 32) yx = u32x4{0, 0, 0, 0};
  }
#define KSTEP(g, CUR, NXT)                                                                                   \
  {                                                                                                          \
    const char* np = lp + ((g) == 31 ? 5 * 28672 : ((((g) + 1) / 4) % 5) * 28672 + (((g) + 1) % 4) * 7168);  \
    _Pragma("unroll") for (int i = 0; i < 7; ++i) NXT[i] = *(const u32x4*)(np + i * 1024);                   \
    _Pragma("unroll") for (int i = 0; i < 7; ++i) acc[i] = mfma(CUR[i], y[g], acc[i]);                       \
  }
#define CHUNK(c, PRE)                                                                                        \
  KSTEP(4 * (c), wc, wn) KSTEP(4 * (c) + 1, wn, wc) KSTEP(4 * (c) + 2, wc, wn) PRE KSTEP(4 * (c) + 3, wn, wc)
  CHUNK(0, WAITB(33, 1);)
  CHUNK(1, WAITB(22, 2); ISSUE(5))
  CHUNK(2, WAITB(22, 3); ISSUE(6))
  CHUNK(3, WAITB(22, 4); ISSUE(7))
  CHUNK(4, WAITB(22, 5);)
  CHUNK(5, WAITB(11, 6);)
  CHUNK(6, WAITB(0, 7);)
  CHUNK(7, )
  TS(4, 2);
  char* hp = ws + WS_HHF + ((size_t)(rt * 112 + t0) * 64 + lane) * 16;
#pragma unroll
  for (int i = 0; i < 7; ++i) {
    const f32x16 a = mfma(wc[i], yx, acc[i]);
    float hv[8];
#pragma unroll
    for (int j = 0; j < 8; ++j) hv[j] = fmaxf(a[j], 0.f) * r2 * a[j + 8];
    *(u32x4*)(hp + i * 1024) = u32x4{pack2(hv[0], hv[1]), pack2(hv[2], hv[3]), pack2(hv[4], hv[5]), pack2(hv[6], hv[7])};
  }
  TS(4, 6);
}

extern "C" void kernel_launch(void* const* d_in, const int* in_sizes, int n_in, void* d_out, int out_size, void* d_ws,
                              size_t ws_size, hipStream_t stream) {
  const u16* x = (const u16*)d_in[0];
  const u16* wq = (const u16*)d_in[1];
  const u16* wk = (const u16*)d_in[2];
  const u16* wv = (const u16*)d_in[3];
  const u16* wo = (const u16*)d_in[4];
  const u16* wg = (const u16*)d_in[6];
  const u16* wu = (const u16*)d_in[7];
  const u16* wd = (const u16*)d_in[8];
  const u16* cc = (const u16*)d_in[9];
  const u16* wmod = (const u16*)d_in[10];
  char* ws = (char*)d_ws;
    k1_prep<<<232, 256, 0, stream>>>(wq, wk, wv, cc, wmod, ws);
  k2_qkv<<<376, 512, 0, stream>>>(x, wg, wu, ws);
  k3_attn<<<256, 1024, 0, stream>>>(wo, wg, wu, wd, ws);
  k46_gemm<32, 8, false><<<256, 256, 0, stream>>>(x, ws + WS_OF, ws + WS_WOF, ws, (u16*)(ws + WS_X1));
  k5_ffn<<<256, 256, 0, stream>>>(ws);
  k46_gemm<112, 10, true><<<256, 256, 0, stream>>>((const u16*)(ws + WS_X1), ws + WS_HHF, ws + WS_WDF, ws, (u16*)d_out);
}
```

```cpp
#include <hip/hip_runtime.h>
#include <hip/hip_bf16.h>
#include <stdint.h>

#define DEV __device__ __forceinline__
typedef __bf16 bf16x8 __attribute__((ext_vector_type(8)));
typedef __bf16 bf16x2 __attribute__((ext_vector_type(2)));
typedef float f32x16 __attribute__((ext_vector_type(16)));
typedef float f32x4 __attribute__((ext_vector_type(4)));
typedef float f32x2 __attribute__((ext_vector_type(2)));
typedef unsigned u32x4 __attribute__((ext_vector_type(4)));
typedef unsigned u32x2 __attribute__((ext_vector_type(2)));
typedef unsigned short u16;

#define K4W 8
#define K4D 4
#define K6W 4
#define K6D 9
#define K5B 1
#define K6B 2
#define WS_MODP 0x0u
#define WS_MODF 0xC000u
#define WS_STAT 0x10000u
#define WS_WKVF 0x30000u
#define WS_KF 0x40000u
#define WS_VF 0x50000u
#define WS_WQF 0x80000u
#define WS_WOF 0x100000u
#define WS_WGUF 0x200000u
#define WS_WDF 0x600000u
#define WS_QF 0x800000u
#define WS_OF 0xA00000u
#define WS_X1 0xC00000u
#define WS_YF 0xE00000u
#define WS_HHF 0x1000000u

#define TS(k, slot)
DEV unsigned pack2(float a, float b) {
  f32x2 v = {a, b};
  bf16x2 r = __builtin_convertvector(v, bf16x2);
  return __builtin_bit_cast(unsigned, r);
}
DEV float bflo(unsigned u) { return __uint_as_float(u << 16); }
DEV float bfhi(unsigned u) { return __uint_as_float(u & 0xffff0000u); }
DEV f32x16 mfma(u32x4 a, u32x4 b, f32x16 c) {
  return __builtin_amdgcn_mfma_f32_32x32x16_bf16(__builtin_bit_cast(bf16x8, a), __builtin_bit_cast(bf16x8, b), c, 0, 0, 0);
}
template <int CTRL> DEV float dppf(float v) {
  return __builtin_bit_cast(float, __builtin_amdgcn_update_dpp(0, __builtin_bit_cast(int, v), CTRL, 0xf, 0xf, true));
}
DEV float red4(float v) { v += dppf<0xB1>(v); v += dppf<0x4E>(v); return v; }
DEV float red8(float v) { v = red4(v); v += dppf<0x141>(v); return v; }
DEV float red16(float v) { v = red8(v); v += dppf<0x140>(v); return v; }
DEV float rl(float v, int l) { return __builtin_bit_cast(float, __builtin_amdgcn_readlane(__builtin_bit_cast(int, v), l)); }
DEV float wave_sum(float v) { v = red16(v); return (rl(v, 0) + rl(v, 16)) + (rl(v, 32) + rl(v, 48)); }
#define GLD(dst, voff, sptr, imm) \
  asm volatile("global_load_dwordx4 %0, %1, %2 offset:%3" : "=v"(dst) : "v"(voff), "s"(sptr), "n"(imm) : "memory")
#define GLD1(dst, voff, sptr, imm) \
  asm volatile("global_load_dword %0, %1, %2 offset:%3" : "=v"(dst) : "v"(voff), "s"(sptr), "n"(imm) : "memory")
DEV void dma16(unsigned voff, const char* sptr, unsigned ldsoff) {
  asm volatile("s_mov_b32 m0, %2\n\ts_nop 0\n\tglobal_load_lds_dwordx4 %0, %1" ::"v"(voff), "s"(sptr), "s"(ldsoff) : "memory");
}
DEV void st16(void* p, u32x4 v) { asm volatile("global_store_dwordx4 %0, %1, off sc1" ::"v"(p), "v"(v) : "memory"); }
DEV f32x16 zero16() { f32x16 z; for (int i = 0; i < 16; ++i) z[i] = 0.f; return z; }

__global__ __launch_bounds__(256) void k1_prep(const u16* __restrict__ wq, const u16* __restrict__ wk, const u16* __restrict__ wv,
                                               const u16* __restrict__ cc, const u16* __restrict__ wmod, char* __restrict__ ws) {
  __shared__ __attribute__((aligned(16))) u16 tile[64 * 136];
  const int tid = threadIdx.x, lane = tid & 63, w = tid >> 6;
  int b = blockIdx.x;
  TS(0, 0);
  if (b < 192) {
    const int cbk = b % 48, kq = b / 48;
    const int c8 = tid & 7, kg = tid >> 3;
    const int k0 = kq * 128 + kg * 4;
    u32x4 wr[4];
#pragma unroll
    for (int i = 0; i < 4; ++i) wr[i] = *(const u32x4*)(wmod + (size_t)(k0 + i) * 3072 + cbk * 64 + c8 * 8);
    u32x2 cr = *(const u32x2*)(cc + k0);
    float cv[4] = {bflo(cr[0]), bfhi(cr[0]), bflo(cr[1]), bfhi(cr[1])};
    float acc[8];
#pragma unroll
    for (int j = 0; j < 8; ++j) acc[j] = 0.f;
#pragma unroll
    for (int i = 0; i < 4; ++i) {
      float s = cv[i] / (1.f + __expf(-cv[i]));
#pragma unroll
      for (int j = 0; j < 4; ++j) { acc[2 * j] += s * bflo(wr[i][j]); acc[2 * j + 1] += s * bfhi(wr[i][j]); }
    }
    float* part = (float*)tile;
#pragma unroll
    for (int j = 0; j < 8; ++j) acc[j] += dppf<0x128>(acc[j]);
    if ((lane & 15) < 8) {
      float* p = part + (w * 4 + (lane >> 4)) * 64 + c8 * 8;
      *(f32x4*)p = f32x4{acc[0], acc[1], acc[2], acc[3]};
      *(f32x4*)(p + 4) = f32x4{acc[4], acc[5], acc[6], acc[7]};
    }
    __syncthreads();
    {
      const int col = tid >> 2, seg = tid & 3;
      float s = 0.f;
#pragma unroll
      for (int i = 0; i < 4; ++i) s += part[(seg * 4 + i) * 64 + col];
      s = red4(s);
      if (seg == 0) ((float*)(ws + WS_MODP))[kq * 3072 + cbk * 64 + col] = s;
    }
    TS(0, 7);
    return;
  }
  b -= 192;
  if (b < 8) {
    const int ks = b * 4 + w, h = lane >> 5, m = lane & 31;
    const u16* src = (m < 16 ? wk : wv) + (m & 15) + (ks * 16 + h * 8) * 16;
    u32x4 o;
#pragma unroll
    for (int j = 0; j < 4; ++j) o[j] = (unsigned)src[(2 * j) * 16] | ((unsigned)src[(2 * j + 1) * 16] << 16);
    *(u32x4*)(ws + WS_WKVF + (ks * 64 + lane) * 16) = o;
    return;
  }
  b -= 8;
  const int kb = b >> 2, nb = b & 3;
  const u16* sl = wq + kb * 64 * 512 + nb * 128;
  u32x4 r[4];
#pragma unroll
  for (int i = 0; i < 4; ++i) {
    const int p = tid + 256 * i, row = p >> 4, c = p & 15;
    r[i] = *(const u32x4*)(sl + row * 512 + c * 8);
  }
#pragma unroll
  for (int i = 0; i < 4; ++i) {
    const int p = tid + 256 * i, row = p >> 4, c = p & 15;
    *(u32x4*)(tile + row * 136 + c * 8) = r[i];
  }
  __syncthreads();
  const int h = lane >> 5, m = lane & 31;
  const int ka = w * 16 + 8 * h, kb2 = ka + 4;
#pragma unroll
  for (int tt = 0; tt < 4; ++tt) {
    const int n = tt * 32 + m;
    const u16* pa = tile + ka * 136 + n;
    const u16* pb = tile + kb2 * 136 + n;
    u32x4 o;
    o[0] = (unsigned)pa[0] | ((unsigned)pa[136] << 16);
    o[1] = (unsigned)pa[272] | ((unsigned)pa[408] << 16);
    o[2] = (unsigned)pb[0] | ((unsigned)pb[136] << 16);
    o[3] = (unsigned)pb[272] | ((unsigned)pb[408] << 16);
    *(u32x4*)(ws + WS_WQF + ((size_t)((nb * 4 + tt) * 32 + kb * 4 + w) * 64 + lane) * 16) = o;
  }
  TS(0, 6);
}

DEV void split_bf(float v, unsigned& hi, unsigned& lo) {
  unsigned p = pack2(v, 0.f) & 0xffffu;
  float r = v - __uint_as_float(p << 16);
  hi = p; lo = pack2(r, 0.f) & 0xffffu;
}

template <bool iskv> DEV void qkv_body(const u16* __restrict__ x, char* __restrict__ ws, char* smem, const int rt, const int cb) {
  const int tid = threadIdx.x, lane = tid & 63;
  const int w = __builtin_amdgcn_readfirstlane(tid >> 6);
  const float* modp = (const float*)(ws + WS_MODP);
  float* sc = (float*)(smem + 33792 + 32768);
  const unsigned l16 = lane * 16;
  u32x4 xr[4];
  {
    const char* xp = (const char*)(x + (size_t)(rt * 32 + w * 4) * 512);
    GLD(xr[0], l16, xp, 0); GLD(xr[1], l16, xp, 1024); GLD(xr[2], l16, xp, 2048); GLD(xr[3], l16, xp, 3072);
  }
  float mp[8];
#pragma unroll
  for (int i = 0; i < 4; ++i) { GLD1(mp[i], tid * 4, modp + i * 3072, 0); GLD1(mp[4 + i], tid * 4, modp + i * 3072, 2048); }
  u32x4 wf[16];
  {
    const char* p = iskv ? ws + WS_WKVF + w * 4096 : ws + WS_WQF + (size_t)((cb * 4 + (w & 3)) * 32 + (w >> 2) * 16) * 1024;
    GLD(wf[0], l16, p, 0); GLD(wf[1], l16, p, 1024); GLD(wf[2], l16, p, 2048); GLD(wf[3], l16, p, 3072);
    if (!iskv) {
#pragma unroll
      for (int i = 1; i < 4; ++i) {
        const char* q = p + i * 4096;
        GLD(wf[4 * i], l16, q, 0); GLD(wf[4 * i + 1], l16, q, 1024); GLD(wf[4 * i + 2], l16, q, 2048); GLD(wf[4 * i + 3], l16, q, 3072);
      }
      asm volatile("s_waitcnt vmcnt(16)" : "+v"(xr[0]), "+v"(xr[1]), "+v"(xr[2]), "+v"(xr[3]), "+v"(mp[0]), "+v"(mp[1]), "+v"(mp[2]),
                   "+v"(mp[3]), "+v"(mp[4]), "+v"(mp[5]), "+v"(mp[6]), "+v"(mp[7]));
    } else {
      asm volatile("s_waitcnt vmcnt(4)" : "+v"(xr[0]), "+v"(xr[1]), "+v"(xr[2]), "+v"(xr[3]), "+v"(mp[0]), "+v"(mp[1]), "+v"(mp[2]),
                   "+v"(mp[3]), "+v"(mp[4]), "+v"(mp[5]), "+v"(mp[6]), "+v"(mp[7]));
    }
  }
  TS(1, 1);
  sc[tid] = (mp[0] + mp[1]) + (mp[2] + mp[3]);
  sc[512 + tid] = 1.f + ((mp[4] + mp[5]) + (mp[6] + mp[7]));
  float v[4][8];
#pragma unroll
  for (int i = 0; i < 4; ++i) {
#pragma unroll
    for (int j = 0; j < 4; ++j) { v[i][2 * j] = bflo(xr[i][j]); v[i][2 * j + 1] = bfhi(xr[i][j]); }
    float s = ((v[i][0] + v[i][1]) + (v[i][2] + v[i][3])) + ((v[i][4] + v[i][5]) + (v[i][6] + v[i][7]));
    const float mean = wave_sum(s) * (1.f / 512.f);
    float q = 0.f;
#pragma unroll
    for (int j = 0; j < 8; ++j) { v[i][j] -= mean; q += v[i][j] * v[i][j]; }
    const float r = 1.f / sqrtf(wave_sum(q) * (1.f / 512.f) + 1e-5f);
#pragma unroll
    for (int j = 0; j < 8; ++j) v[i][j] *= r;
  }
  asm volatile("s_waitcnt lgkmcnt(0)\n\ts_barrier" ::: "memory");
  {
    const f32x4 s0 = *(const f32x4*)(sc + lane * 8), s1 = *(const f32x4*)(sc + lane * 8 + 4);
    const f32x4 c0 = *(const f32x4*)(sc + 512 + lane * 8), c1 = *(const f32x4*)(sc + 512 + lane * 8 + 4);
#pragma unroll
    for (int i = 0; i < 4; ++i) {
      u32x4 o;
      o[0] = pack2(v[i][0] * c0[0] + s0[0], v[i][1] * c0[1] + s0[1]);
      o[1] = pack2(v[i][2] * c0[2] + s0[2], v[i][3] * c0[3] + s0[3]);
      o[2] = pack2(v[i][4] * c1[0] + s1[0], v[i][5] * c1[1] + s1[1]);
      o[3] = pack2(v[i][6] * c1[2] + s1[2], v[i][7] * c1[3] + s1[3]);
      *(u32x4*)(smem + lane * 528 + (w * 4 + i) * 16) = o;
    }
  }
  asm volatile("s_waitcnt vmcnt(0)" : "+v"(wf[0]), "+v"(wf[1]), "+v"(wf[2]), "+v"(wf[3]));
  if (!iskv)
    asm volatile("" : "+v"(wf[4]), "+v"(wf[5]), "+v"(wf[6]), "+v"(wf[7]), "+v"(wf[8]), "+v"(wf[9]), "+v"(wf[10]), "+v"(wf[11]),
                 "+v"(wf[12]), "+v"(wf[13]), "+v"(wf[14]), "+v"(wf[15]));
  if (iskv && rt < 6) {
    const int i = rt * 512 + tid;
    ((float*)(ws + WS_MODF))[i] = (modp[i] + modp[3072 + i]) + (modp[6144 + i] + modp[9216 + i]);
  }
  __syncthreads();
  TS(1, 2);
  const char* hb = smem + (lane >> 5) * 528 + (lane & 31) * 16;
  char* red = smem + 33792;
  f32x16 acc = zero16();
  if (iskv) {
#pragma unroll
    for (int i = 0; i < 4; ++i) acc = mfma(wf[i], *(const u32x4*)(hb + (w * 4 + i) * 1056), acc);
#pragma unroll
    for (int q = 0; q < 4; ++q)
      *(f32x4*)(red + (w * 4 + q) * 1024 + lane * 16) = f32x4{acc[4 * q], acc[4 * q + 1], acc[4 * q + 2], acc[4 * q + 3]};
    __syncthreads();
    if (tid < 256) {
      const int q = w;
      f32x4 s = {0, 0, 0, 0};
#pragma unroll
      for (int p = 0; p < 8; ++p) s += *(const f32x4*)(red + (p * 4 + q) * 1024 + lane * 16);
      const unsigned p0 = pack2(s[0], s[1]), p1 = pack2(s[2], s[3]);
      if (q < 2) {
        *(u32x2*)(ws + WS_KF + rt * 1024 + lane * 16 + q * 8) = u32x2{p0, p1};
      } else {
        const int t = lane & 31, h = lane >> 5;
        const int d0 = 8 * (q - 2) + 4 * h;
        u16* vp = (u16*)(ws + WS_VF + rt * 1024 + (t >> 4) * 512 + ((t >> 2) & 1) * 256 + d0 * 16) + (((t >> 3) & 1) * 4 + (t & 3));
        vp[0] = (u16)p0; vp[8] = (u16)(p0 >> 16); vp[16] = (u16)p1; vp[24] = (u16)(p1 >> 16);
      }
    }
  } else {
    const int kh = w >> 2, ct = w & 3;
#pragma unroll
    for (int i = 0; i < 16; ++i) acc = mfma(wf[i], *(const u32x4*)(hb + (kh * 16 + i) * 1056), acc);
    f32x4 k0, k1, g0, g1;
    if (kh == 0) {
      k0 = f32x4{acc[0], acc[1], acc[2], acc[3]}; k1 = f32x4{acc[4], acc[5], acc[6], acc[7]};
      g0 = f32x4{acc[8], acc[9], acc[10], acc[11]}; g1 = f32x4{acc[12], acc[13], acc[14], acc[15]};
    } else {
      g0 = f32x4{acc[0], acc[1], acc[2], acc[3]}; g1 = f32x4{acc[4], acc[5], acc[6], acc[7]};
      k0 = f32x4{acc[8], acc[9], acc[10], acc[11]}; k1 = f32x4{acc[12], acc[13], acc[14], acc[15]};
    }
    *(f32x4*)(red + w * 2048 + lane * 16) = g0;
    *(f32x4*)(red + w * 2048 + 1024 + lane * 16) = g1;
    __syncthreads();
    k0 += *(const f32x4*)(red + (w ^ 4) * 2048 + lane * 16);
    k1 += *(const f32x4*)(red + (w ^ 4) * 2048 + 1024 + lane * 16);
    const float sc = 0.25f * 1.4426950408889634f;
    k0 *= sc; k1 *= sc;
    u32x4 o = {pack2(k0[0], k0[1]), pack2(k0[2], k0[3]), pack2(k1[0], k1[1]), pack2(k1[2], k1[3])};
    const int head = (cb * 4 + ct) * 2 + kh;
    *(u32x4*)(ws + WS_QF + ((size_t)(rt * 32 + head) * 64 + lane) * 16) = o;
  }
}

__global__ __launch_bounds__(512, 4) void k2_qkv(const u16* __restrict__ x, const u16* __restrict__ wg, const u16* __restrict__ wu,
                                                 char* __restrict__ ws) {
  __shared__ __attribute__((aligned(16))) char smem[33792 + 32768 + 4096];
  const int tid = threadIdx.x, lane = tid & 63;
  const int w = __builtin_amdgcn_readfirstlane(tid >> 6);
  const float* modp = (const float*)(ws + WS_MODP);
  float* sc = (float*)(smem + 33792 + 32768);
  int b = blockIdx.x;
  TS(1, 0);
  if (b < 56) {
    const int up = b >= 28, f0 = (b % 28) * 64;
    const int c8 = tid & 7, kg = tid >> 3;
    const u16* src = (up ? wu : wg) + (size_t)(kg * 8) * 1792 + f0 + c8 * 8;
    u32x4 wr[8];
#pragma unroll
    for (int i = 0; i < 8; ++i) wr[i] = *(const u32x4*)(src + i * 1792);
    {
      const float* mp = modp + 1536 + tid;
      sc[tid] = (mp[0] + mp[3072]) + (mp[6144] + mp[9216]);
      sc[512 + tid] = 1.f + ((mp[512] + mp[3584]) + (mp[6656] + mp[9728]));
    }
    __syncthreads();
    const f32x4 s0 = *(const f32x4*)(sc + kg * 8), s1 = *(const f32x4*)(sc + kg * 8 + 4);
    const f32x4 c0 = *(const f32x4*)(sc + 512 + kg * 8), c1 = *(const f32x4*)(sc + 512 + kg * 8 + 4);
    float au[8], ab[8];
#pragma unroll
    for (int j = 0; j < 8; ++j) au[j] = ab[j] = 0.f;
#pragma unroll
    for (int i = 0; i < 8; ++i) {
      const float c = i < 4 ? c0[i & 3] : c1[i & 3], s = i < 4 ? s0[i & 3] : s1[i & 3];
#pragma unroll
      for (int j = 0; j < 4; ++j) {
        const float lo = bflo(wr[i][j]), hi = bfhi(wr[i][j]);
        au[2 * j] += c * lo; au[2 * j + 1] += c * hi; ab[2 * j] += s * lo; ab[2 * j + 1] += s * hi;
      }
    }
#pragma unroll
    for (int j = 0; j < 8; ++j) { au[j] += dppf<0x128>(au[j]); ab[j] += dppf<0x128>(ab[j]); }
    float* part = (float*)smem;
    if ((lane & 15) < 8) {
      float* p = part + (w * 4 + (lane >> 4)) * 128 + c8 * 16;
      *(f32x4*)p = f32x4{au[0], au[1], au[2], au[3]};
      *(f32x4*)(p + 4) = f32x4{au[4], au[5], au[6], au[7]};
      *(f32x4*)(p + 8) = f32x4{ab[0], ab[1], ab[2], ab[3]};
      *(f32x4*)(p + 12) = f32x4{ab[4], ab[5], ab[6], ab[7]};
    }
    __syncthreads();
    const int col = tid >> 3, seg = tid & 7;
    float u = 0.f, bb = 0.f;
#pragma unroll
    for (int i = 0; i < 4; ++i) {
      const float* p = part + (seg * 4 + i) * 128 + (col >> 3) * 16 + (col & 7);
      u += p[0]; bb += p[8];
    }
    u = red8(u); bb = red8(bb);
    if (seg < 2) {
      unsigned uh, ul, bh, bl;
      split_bf(u, uh, ul); split_bf(bb, bh, bl);
      u32x4 o = {uh | (ul << 16), uh | (ul << 16), bh | (bl << 16), bh | (bl << 16)};
      if (seg) o = u32x4{0, 0, 0, 0};
      const int f = f0 + col, t = f >> 4, m = (f & 15) + 16 * up + 32 * seg;
      *(u32x4*)(ws + WS_WGUF + ((size_t)(t * 33 + 32) * 64 + m) * 16) = o;
    }
    TS(1, 7);
    return;
  }
  b -= 56;
  if (b < 64) qkv_body<true>(x, ws, smem, b, 0);
  else qkv_body<false>(x, ws, smem, b & 63, (b >> 6) - 1);
  TS(1, 6);
}

DEV float xhalf_max(float m) { return fmaxf(m, __shfl_xor(m, 32)); }
DEV float max16(const f32x16& s) {
  float a = fmaxf(fmaxf(s[0], s[1]), s[2]), b = fmaxf(fmaxf(s[3], s[4]), s[5]), c = fmaxf(fmaxf(s[6], s[7]), s[8]);
  float d = fmaxf(fmaxf(s[9], s[10]), s[11]), e = fmaxf(fmaxf(s[12], s[13]), s[14]);
  return fmaxf(fmaxf(fmaxf(a, b), c), fmaxf(fmaxf(d, e), s[15]));
}
#define ATT_LOOP \
  "1:\n\tv_exp_f32 v0, v0\n\tv_exp_f32 v1, v1\n\tv_exp_f32 v2, v2\n\t" \
  "v_exp_f32 v3, v3\n\tv_mfma_f32_32x32x16_bf16 v[48:63], v[88:91], v[96:99], v[48:63]\n\ts_waitcnt lgkmcnt(0)\n\tds_read_b128 v[68:71], v100 offset:2048\n\t" \
  "s_mov_b64 exec, %[msk]\n\tds_read_b128 v[84:87], v101 offset:1024\n\tds_read_b128 v[88:91], v101 offset:1536\n\ts_mov_b64 exec, -1\n\t" \
  "v_cvt_pk_bf16_f32 v92, v0, v1\n\tv_exp_f32 v4, v4\n\tv_exp_f32 v5, v5\n\tv_mfma_f32_32x32x16_bf16 v[16:31], v[72:75], v[64:67], v[32:47]\n\t" \
  "v_cvt_pk_bf16_f32 v93, v2, v3\n\tv_exp_f32 v6, v6\n\tv_exp_f32 v7, v7\n\tv_cvt_pk_bf16_f32 v94, v4, v5\n\t" \
  "v_exp_f32 v8, v8\n\tv_exp_f32 v9, v9\n\tv_cvt_pk_bf16_f32 v95, v6, v7\n\tv_exp_f32 v10, v10\n\t" \
  "v_exp_f32 v11, v11\n\tv_mfma_f32_32x32x16_bf16 v[48:63], v[76:79], v[92:95], v[48:63]\n\tv_cvt_pk_bf16_f32 v96, v8, v9\n\tv_exp_f32 v12, v12\n\t" \
  "v_exp_f32 v13, v13\n\tv_cvt_pk_bf16_f32 v97, v10, v11\n\tv_exp_f32 v14, v14\n\tv_exp_f32 v15, v15\n\t" \
  "v_cvt_pk_bf16_f32 v98, v12, v13\n\tv_cvt_pk_bf16_f32 v99, v14, v15\n\tv_exp_f32 v16, v16\n\tv_exp_f32 v17, v17\n\t" \
  "v_exp_f32 v18, v18\n\tv_exp_f32 v19, v19\n\tv_mfma_f32_32x32x16_bf16 v[48:63], v[80:83], v[96:99], v[48:63]\n\ts_waitcnt lgkmcnt(0)\n\t" \
  "ds_read_b128 v[72:75], v100 offset:3072\n\ts_mov_b64 exec, %[msk]\n\tds_read_b128 v[76:79], v101 offset:2048\n\tds_read_b128 v[80:83], v101 offset:2560\n\t" \
  "s_mov_b64 exec, -1\n\tv_cvt_pk_bf16_f32 v92, v16, v17\n\tv_exp_f32 v20, v20\n\tv_exp_f32 v21, v21\n\t" \
  "v_mfma_f32_32x32x16_bf16 v[0:15], v[68:71], v[64:67], v[32:47]\n\tv_cvt_pk_bf16_f32 v93, v18, v19\n\tv_exp_f32 v22, v22\n\tv_exp_f32 v23, v23\n\t" \
  "v_cvt_pk_bf16_f32 v94, v20, v21\n\tv_exp_f32 v24, v24\n\tv_exp_f32 v25, v25\n\tv_cvt_pk_bf16_f32 v95, v22, v23\n\t" \
  "v_exp_f32 v26, v26\n\tv_exp_f32 v27, v27\n\tv_mfma_f32_32x32x16_bf16 v[48:63], v[84:87], v[92:95], v[48:63]\n\tv_add_u32 v100, 0x800, v100\n\t" \
  "v_add_u32 v101, 0x800, v101\n\ts_sub_u32 %[n], %[n], 1\n\ts_cmp_lg_u32 %[n], 0\n\tv_cvt_pk_bf16_f32 v96, v24, v25\n\t" \
  "v_exp_f32 v28, v28\n\tv_exp_f32 v29, v29\n\tv_cvt_pk_bf16_f32 v97, v26, v27\n\tv_exp_f32 v30, v30\n\t" \
  "v_exp_f32 v31, v31\n\tv_cvt_pk_bf16_f32 v98, v28, v29\n\tv_cvt_pk_bf16_f32 v99, v30, v31\n\ts_cbranch_scc1 1b\n\t" \
  "s_nop 1\n\tv_mfma_f32_32x32x16_bf16 v[48:63], v[88:91], v[96:99], v[48:63]\n\ts_nop 7\n\tv_mov_b32 v96, 0\n\t" \
  "v_mov_b32 v97, 0\n\tv_mov_b32 v98, 0\n\tv_mov_b32 v99, 0\n\ts_waitcnt lgkmcnt(0)\n\t" \
  "s_nop 15\n\t\n\t" \
  ""
#define LDK(dst, addr, off) asm volatile("ds_read_b128 %0, %1 offset:" #off : "=v"(dst) : "v"(addr))
#define LDV(d0, d1, addr, off0, off1)                                                                                   \
  asm volatile("s_mov_b64 exec, %3\n\tds_read_b128 %0, %2 offset:" #off0 "\n\tds_read_b128 %1, %2 offset:" #off1        \
               "\n\ts_mov_b64 exec, -1"                                                                                 \
               : "+v"(d0), "+v"(d1)                                                                                     \
               : "v"(addr), "s"(vmask))
#define LWAIT(a, b, c) asm volatile("s_waitcnt lgkmcnt(0)" : "+v"(a), "+v"(b), "+v"(c))

#define LDH(d0, d1, p) \
  asm volatile("global_load_ushort %0, %2, off\n\tglobal_load_ushort %1, %2, off offset:64" : "=&v"(d0), "=&v"(d1) : "v"(p) : "memory")
__global__ __launch_bounds__(1024, 4) void k3_attn(const u16* __restrict__ wo, const u16* __restrict__ wg, const u16* __restrict__ wu,
                                                   const u16* __restrict__ wd, char* __restrict__ ws) {
  __shared__ __attribute__((aligned(16))) char smem[131072 + 4096];
  const int tid = threadIdx.x, lane = tid & 63;
  const int w = __builtin_amdgcn_readfirstlane(tid >> 6);
  const int rt = blockIdx.x & 63, hg = blockIdx.x >> 6;
  TS(2, 0);
  const int kh = w >> 3;
  const unsigned sbase = (unsigned)(uintptr_t)smem;
  const int head = hg * 8 + (w & 7);
  u32x4 q;
  {
    const unsigned l16 = lane * 16;
    GLD(q, l16, ws + WS_QF + (size_t)(rt * 32 + head) * 1024, 0);
    const char* kp = ws + WS_KF + kh * 32768 + (w & 7) * 1024;
    const unsigned lb = sbase + kh * 32768 + (w & 7) * 1024;
#pragma unroll
    for (int i = 0; i < 4; ++i) {
      dma16(l16, kp + i * 8192, lb + i * 8192);
      dma16(l16, kp + i * 8192 + 65536, lb + i * 8192 + 65536);
    }
  }
  unsigned f0[8], f1[8];
  char* fdst;
  unsigned fstep;
  bool fstore;
  {
    int p = blockIdx.x * 12 + (w < 12 ? w : w - 4);
    fstore = w < 12 && p < 2944;
    if (p >= 2944) p -= 128;
    const int h = lane >> 5, m = lane & 31;
    const u16* src;
    int ld, ka, kb2;
    if (p < 1792) {
      const int t = (p >> 6) * 4 + (p & 1), ks = (p >> 1) & 31;
      ld = 1792; ka = 8 * h; kb2 = ka + 4;
      src = (m < 16 ? wg : wu) + ks * 16 * 1792 + t * 16 + (m & 15);
      fdst = ws + WS_WGUF + (size_t)(t * 33 + ks) * 1024; fstep = 2 * 33 * 1024;
    } else {
      const bool isd = p < 2688;
      const int pp = p - (isd ? 1792 : 2688), t = (pp & 7) * 2, ks = pp >> 3, KS = isd ? 112 : 32;
      ld = 512; ka = 4 * h; kb2 = ka + 8;
      src = (isd ? wd : wo) + ks * 16 * 512 + t * 32 + m;
      fdst = ws + (isd ? WS_WDF : WS_WOF) + (size_t)(t * KS + ks) * 1024; fstep = KS * 1024;
    }
    fdst += lane * 16;
    const u16 *pa = src + ka * ld, *pb = src + kb2 * ld;
    const u16* rp[8] = {pa, pa + ld, pa + 2 * ld, pa + 3 * ld, pb, pb + ld, pb + 2 * ld, pb + 3 * ld};
    asm volatile("s_waitcnt vmcnt(6)\n\ts_barrier" : "+v"(q)::"memory");
#pragma unroll
    for (int j = 0; j < 8; ++j) LDH(f0[j], f1[j], rp[j]);
  }
  const unsigned long long vmask = 0x0000ffff0000ffffull;
  const unsigned kbase = sbase + kh * 32768 + lane * 16;
  const unsigned vbase = sbase + 65536 + kh * 32768 + (lane >> 5) * 256 + (lane & 15) * 16;
  const unsigned one1 = (lane & 27) == 16 ? 0x3f803f80u : 0u;
  const u32x4 ones = {one1, one1, one1, one1};
  u32x4 va0 = ones, va1 = ones, vb0 = ones, vb1 = ones, ka, kb;
  TS(2, 1);
  f32x16 o;
  float off;
  for (int attempt = 0; attempt < 2; ++attempt) {
    float m = -3.0e38f;
    const int ntm = attempt ? 32 : 1;
    for (int t = 0; t < ntm; ++t) {
      const unsigned a = kbase + t * 1024;
      LDK(ka, a, 0);
      LWAIT(ka, va0, va1);
      m = fmaxf(m, max16(mfma(ka, q, zero16())));
    }
    m = xhalf_max(m);
    const float thr = attempt ? __builtin_inff() : 1.f;
    off = m + (attempt ? 0.f : 80.f);
    f32x16 c;
#pragma unroll
    for (int i = 0; i < 16; ++i) c[i] = -off;
    o = zero16();
    unsigned kad = kbase, vad = vbase;
    LDK(ka, kad, 0);
    LDK(kb, kad, 1024);
    LDV(va0, va1, vad, 0, 512);
    asm volatile("s_waitcnt lgkmcnt(0)" : "+v"(ka), "+v"(kb), "+v"(va0), "+v"(va1));
    f32x16 s0 = mfma(ka, q, c);
    u32x4 pz = {0, 0, 0, 0};
    for (int seg = 0; seg < 3; ++seg) {
      if (seg == 1 && attempt == 0) asm volatile("s_waitcnt vmcnt(16)\n\ts_barrier" ::: "memory");
      if (seg == 2 && attempt == 0) {
        asm volatile("s_waitcnt vmcnt(0)"
                     : "+v"(f0[0]), "+v"(f0[1]), "+v"(f0[2]), "+v"(f0[3]), "+v"(f0[4]), "+v"(f0[5]), "+v"(f0[6]), "+v"(f0[7]),
                       "+v"(f1[0]), "+v"(f1[1]), "+v"(f1[2]), "+v"(f1[3]), "+v"(f1[4]), "+v"(f1[5]), "+v"(f1[6]), "+v"(f1[7])::"memory");
        if (fstore) {
          const u32x4 g0 = {f0[0] | (f0[1] << 16), f0[2] | (f0[3] << 16), f0[4] | (f0[5] << 16), f0[6] | (f0[7] << 16)};
          const u32x4 g1 = {f1[0] | (f1[1] << 16), f1[2] | (f1[3] << 16), f1[4] | (f1[5] << 16), f1[6] | (f1[7] << 16)};
          asm volatile("global_store_dwordx4 %0, %1, off sc1\n\tglobal_store_dwordx4 %2, %3, off sc1\n\ts_nop 1"
                       ::"v"(fdst), "v"(g0), "v"(fdst + fstep), "v"(g1) : "memory");
        }
      }
      if (seg) {
        if (__any(o[8] > thr)) {
          const int e = __builtin_amdgcn_frexp_expf(o[8]) + 80;
          const float f = (float)e;
#pragma unroll
          for (int i = 0; i < 9; ++i) o[i] = __builtin_amdgcn_ldexpf(o[i], -e);
          off += f;
#pragma unroll
          for (int i = 0; i < 16; ++i) { c[i] = -off; s0[i] -= f; }
        }
      }
      int np = seg == 0 ? 2 : seg == 1 ? 5 : 9;
      asm volatile("s_nop 15\n\t" ATT_LOOP
                   : "+{v[0:15]}"(s0), "+{v[48:63]}"(o), "+{v[72:75]}"(kb), "+{v[76:79]}"(va0), "+{v[80:83]}"(va1),
                     "+{v[84:87]}"(vb0), "+{v[88:91]}"(vb1), "+{v[96:99]}"(pz), "+{v100}"(kad), "+{v101}"(vad), [n] "+s"(np)
                   : "{v[32:47]}"(c), "{v[64:67]}"(q), [msk] "s"(vmask)
                   : "v16", "v17", "v18", "v19", "v20", "v21", "v22", "v23", "v24", "v25", "v26", "v27", "v28", "v29", "v30", "v31",
                     "v68", "v69", "v70", "v71", "v92", "v93", "v94", "v95", "scc", "memory");
    }
    if (attempt) break;
    float a = 0.f, s = o[8];
#pragma unroll
    for (int i = 0; i < 8; ++i) { a = fmaxf(a, fabsf(o[i])); s += o[i] * 0.f; }
    const bool ok = (s < 3.0e38f) && (a >= 7.8886091e-31f);
    if (!__any(!ok)) break;
  }
  TS(2, 2);
  __syncthreads();
  char* ex = smem + (w & 7) * 3072 + lane * 16;
  if (kh) {
    *(f32x4*)(ex) = f32x4{o[0], o[1], o[2], o[3]};
    *(f32x4*)(ex + 1024) = f32x4{o[4], o[5], o[6], o[7]};
    *(f32x2*)(ex + 2048) = f32x2{o[8], off};
  }
  __syncthreads();
  if (!kh) {
    const f32x4 g0 = *(const f32x4*)(ex), g1 = *(const f32x4*)(ex + 1024);
    const f32x2 g2 = *(const f32x2*)(ex + 2048);
    const float m = fmaxf(off, g2[1]);
    float fk = __builtin_amdgcn_exp2f(off - m), fg = __builtin_amdgcn_exp2f(g2[1] - m);
    const float inv = 1.f / (o[8] * fk + g2[0] * fg);
    fk *= inv; fg *= inv;
    const f32x4 k0 = f32x4{o[0], o[1], o[2], o[3]} * fk + g0 * fg, k1 = f32x4{o[4], o[5], o[6], o[7]} * fk + g1 * fg;
    const u32x4 ov = {pack2(k0[0], k0[1]), pack2(k0[2], k0[3]), pack2(k1[0], k1[1]), pack2(k1[2], k1[3])};
    *(u32x4*)(ws + WS_OF + ((size_t)(rt * 32 + head) * 64 + lane) * 16) = ov;
  }
  TS(2, 6);
}

template <int KS, int DEPTH, bool FINAL, int NW>
__global__ __launch_bounds__(NW * 64, K6B) void k46_gemm(const u16* __restrict__ res, const char* __restrict__ af, const char* __restrict__ wf,
                                                   char* __restrict__ ws, u16* __restrict__ out) {
  __shared__ __attribute__((aligned(16))) float red[NW * 64 * 68];
  const int tid = threadIdx.x, lane = tid & 63;
  const int w = __builtin_amdgcn_readfirstlane(tid >> 6);
  const int b = blockIdx.x;
  const int rb = (b & 7) * 4 + ((b >> 3) & 3), cbk = b >> 5;
  constexpr int N = KS / NW, E = 8 / NW;
  TS(FINAL ? 5 : 3, 0);
  const unsigned l16 = lane * 16;
  const char* ap = af + (size_t)(rb * 2 * KS + w * N) * 1024;
  const char* wp = wf + (size_t)(cbk * 2 * KS + w * N) * 1024;
  u32x4 fa[DEPTH][2], fw[DEPTH][2];
#define LD4(s, k)                                                                                     \
  {                                                                                                   \
    GLD(fa[s][0], l16, ap + ((k) >> 2) * 4096, ((k) & 3) * 1024);                                     \
    GLD(fw[s][0], l16, wp + ((k) >> 2) * 4096, ((k) & 3) * 1024);                                     \
    GLD(fa[s][1], l16, ap + KS * 1024 + ((k) >> 2) * 4096, ((k) & 3) * 1024);                         \
    GLD(fw[s][1], l16, wp + KS * 1024 + ((k) >> 2) * 4096, ((k) & 3) * 1024);                         \
  }
#pragma unroll
  for (int i = 0; i < DEPTH; ++i) LD4(i, i)
  const int cg = tid & 7, r0 = tid >> 3;
  const int col = cbk * 64 + cg * 8;
  const float* modf = (const float*)(ws + WS_MODF);
  u32x4 xr[2], g0u, g1u, c0u, c1u;
  {
    const char* rp = (const char*)(res + (size_t)(rb * 64) * 512 + cbk * 64);
    const unsigned ro = r0 * 1024 + cg * 16;
    GLD(xr[0], ro, rp, 0);
    if (E == 2) { GLD(xr[1], ro, rp + 32768, 0); } else xr[1] = u32x4{0, 0, 0, 0};
    const unsigned mo = cg * 32;
    const char* gp = (const char*)(modf + (FINAL ? 2560 : 1024) + cbk * 64);
    GLD(g0u, mo, gp, 0); GLD(g1u, mo, gp, 16);
    const char* cp = (const char*)(modf + 2048 + cbk * 64);
    GLD(c0u, mo, cp, 0); GLD(c1u, mo, cp, 16);
  }
  f32x16 acc[2][2];
  acc[0][0] = acc[0][1] = acc[1][0] = acc[1][1] = zero16();
#pragma unroll
  for (int k = 0; k < N; ++k) {
    const int s = k % DEPTH;
    const int after = 4 * ((k + DEPTH < N ? k + DEPTH : N) - k - 1) + (k < DEPTH ? 4 + E : 0);
    asm volatile("s_waitcnt vmcnt(%4)" : "+v"(fa[s][0]), "+v"(fa[s][1]), "+v"(fw[s][0]), "+v"(fw[s][1]) : "n"(after));
    if (k == 0) TS(FINAL ? 5 : 3, 1);
    acc[0][0] = mfma(fw[s][0], fa[s][0], acc[0][0]);
    acc[0][1] = mfma(fw[s][0], fa[s][1], acc[0][1]);
    acc[1][0] = mfma(fw[s][1], fa[s][0], acc[1][0]);
    acc[1][1] = mfma(fw[s][1], fa[s][1], acc[1][1]);
    if (k + DEPTH < N) LD4(s, k + DEPTH)
  }
  asm volatile("s_waitcnt vmcnt(0)" : "+v"(xr[0]), "+v"(xr[1]), "+v"(g0u), "+v"(g1u), "+v"(c0u), "+v"(c1u));
  TS(FINAL ? 5 : 3, 2);
  const f32x4 g0 = __builtin_bit_cast(f32x4, g0u), g1 = __builtin_bit_cast(f32x4, g1u);
  const f32x4 c0 = __builtin_bit_cast(f32x4, c0u) + 1.f, c1 = __builtin_bit_cast(f32x4, c1u) + 1.f;
  {
    float* rp = red + w * (64 * 68) + (lane & 31) * 68 + (lane >> 5) * 4;
#pragma unroll
    for (int i = 0; i < 2; ++i)
#pragma unroll
      for (int j = 0; j < 2; ++j)
#pragma unroll
        for (int q = 0; q < 4; ++q)
          *(f32x4*)(rp + j * 32 * 68 + i * 32 + q * 8) =
              f32x4{acc[i][j][4 * q], acc[i][j][4 * q + 1], acc[i][j][4 * q + 2], acc[i][j][4 * q + 3]};
  }
  __syncthreads();
#pragma unroll
  for (int e = 0; e < E; ++e) {
    const int r = r0 + 32 * e, row = rb * 64 + r;
    f32x4 s0 = {0, 0, 0, 0}, s1 = s0;
#pragma unroll
    for (int p = 0; p < NW; ++p) {
      const float* rp = red + p * (64 * 68) + r * 68 + cg * 8;
      s0 += *(const f32x4*)rp; s1 += *(const f32x4*)(rp + 4);
    }
    const f32x4 x0 = {bflo(xr[e][0]), bfhi(xr[e][0]), bflo(xr[e][1]), bfhi(xr[e][1])};
    const f32x4 x1 = {bflo(xr[e][2]), bfhi(xr[e][2]), bflo(xr[e][3]), bfhi(xr[e][3])};
    s0 = x0 + g0 * s0; s1 = x1 + g1 * s1;
    const u32x4 o = {pack2(s0[0], s0[1]), pack2(s0[2], s0[3]), pack2(s1[0], s1[1]), pack2(s1[2], s1[3])};
    *(u32x4*)(out + (size_t)row * 512 + col) = o;
    if (!FINAL) {
      const f32x4 y0 = s0 * c0, y1 = s1 * c1;
      const u32x4 y = {pack2(y0[0], y0[1]), pack2(y0[2], y0[3]), pack2(y1[0], y1[1]), pack2(y1[2], y1[3])};
      *(u32x4*)(ws + WS_YF + (size_t)((row >> 5) * 32 + (col >> 4)) * 1024 + ((col >> 3) & 1) * 512 + (row & 31) * 16) = y;
      const float mean = red8(((s0[0] + s0[1]) + (s0[2] + s0[3])) + ((s1[0] + s1[1]) + (s1[2] + s1[3]))) * (1.f / 64.f);
      s0 -= mean; s1 -= mean;
      s0 *= s0; s1 *= s1;
      const float m2 = red8(((s0[0] + s0[1]) + (s0[2] + s0[3])) + ((s1[0] + s1[1]) + (s1[2] + s1[3])));
      if (cg == 0) *(f32x2*)(ws + WS_STAT + (size_t)(row * 8 + cbk) * 8) = f32x2{mean, m2};
    }
  }
  TS(FINAL ? 5 : 3, 6);
}

__global__ __launch_bounds__(256, K5B) void k5_ffn(char* __restrict__ ws) {
  __shared__ __attribute__((aligned(16))) char smem[5 * 28672 + 7168];
  const int tid = threadIdx.x, lane = tid & 63;
  const int w = __builtin_amdgcn_readfirstlane(tid >> 6);
  const int b = blockIdx.x, xc = b & 7, idx = b >> 3;
  const int rb = (xc & 1) * 8 + (idx & 7), cbk = (xc >> 1) * 4 + (idx >> 3);
  const int rt = rb * 4 + w, t0 = cbk * 7;
  TS(4, 0);
  const unsigned sbase = (unsigned)(uintptr_t)smem;
  const unsigned l16 = lane * 16;
  const char* wsrc = ws + WS_WGUF + (size_t)t0 * 33 * 1024;
  const char* ysrc = ws + WS_YF + (size_t)rt * 32 * 1024;
  u32x4 st[4], y[32];
  {
    const char* sp = ws + WS_STAT + (size_t)(rt * 32) * 64;
    const unsigned so = (lane & 31) * 64;
    GLD(st[0], so, sp, 0); GLD(st[1], so, sp, 16); GLD(st[2], so, sp, 32); GLD(st[3], so, sp, 48);
    const int i2 = w + 4 < 7 ? w + 4 : 6;
    dma16(l16, wsrc + (w * 33 + 32) * 1024, sbase + 5 * 28672 + w * 1024);
    dma16(l16, wsrc + (i2 * 33 + 32) * 1024, sbase + 5 * 28672 + i2 * 1024);
  }
#define ISSUE(c)                                                                                             \
  {                                                                                                          \
    _Pragma("unroll") for (int i = 0; i < 7; ++i)                                                            \
        dma16(l16, wsrc + (i * 33 + 4 * (c) + w) * 1024, sbase + ((c) % 5) * 28672 + (w * 7 + i) * 1024);    \
    const char* yp = ysrc + (c) * 4096;                                                                      \
    GLD(y[4 * (c)], l16, yp, 0); GLD(y[4 * (c) + 1], l16, yp, 1024);                                         \
    GLD(y[4 * (c) + 2], l16, yp, 2048); GLD(y[4 * (c) + 3], l16, yp, 3072);                                  \
  }
#define WAITB(n, c)                                                                                          \
  asm volatile("s_waitcnt vmcnt(%4)\n\ts_barrier"                                                            \
               : "+v"(y[4 * (c)]), "+v"(y[4 * (c) + 1]), "+v"(y[4 * (c) + 2]), "+v"(y[4 * (c) + 3]) : "n"(n) : "memory")
#define UP 2
  ISSUE(0) ISSUE(1)
  if (UP > 2) ISSUE(2)
  f32x16 acc[7];
#pragma unroll
  for (int i = 0; i < 7; ++i) { acc[i] = zero16(); asm volatile("" : "+v"(acc[i])); }
  const char* lp = smem + lane * 16;
  u32x4 wc[7], wn[7];
  WAITB(11 * (UP - 1), 0);
  asm volatile("" : "+v"(st[0]), "+v"(st[1]), "+v"(st[2]), "+v"(st[3]));
#pragma unroll
  for (int i = 0; i < 7; ++i) wc[i] = *(const u32x4*)(lp + i * 1024);
  u32x4 we[7];
#pragma unroll
  for (int i = 0; i < 7; ++i) we[i] = *(const u32x4*)(lp + 5 * 28672 + i * 1024);
  TS(4, 1);
  float r2;
  u32x4 yx;
  {
    float mb[8], m2 = 0.f, mean = 0.f;
#pragma unroll
    for (int i = 0; i < 4; ++i) {
      mb[2 * i] = __uint_as_float(st[i][0]); mb[2 * i + 1] = __uint_as_float(st[i][2]);
      m2 += __uint_as_float(st[i][1]) + __uint_as_float(st[i][3]);
      mean += mb[2 * i] + mb[2 * i + 1];
    }
    mean *= 0.125f;
    float d = 0.f;
#pragma unroll
    for (int i = 0; i < 8; ++i) d += (mb[i] - mean) * (mb[i] - mean);
    const float var = (m2 + 64.f * d) * (1.f / 512.f) + 1e-5f;
    r2 = 1.f / var;
    unsigned mh, ml, rh, rlo;
    split_bf(-mean, mh, ml); split_bf(sqrtf(var), rh, rlo);
    yx = u32x4{mh | (mh << 16), ml | (ml << 16), rh | (rh << 16), rlo | (rlo << 16)};
    if (lane >= 32) yx = u32x4{0, 0, 0, 0};
  }
#define MFV(c, a, b) asm volatile("v_mfma_f32_32x32x16_bf16 %0, %1, %2, %0" : "+v"(c) : "v"(a), "v"(b))
#define KSTEP(g, CUR, NXT, ic, ib)                                                                                   \
  {                                                                                                          \
    const char* np = lp + ((((g) + 1) / 4) % 5) * 28672 + (((g) + 1) % 4) * 7168;                            \
    _Pragma("unroll") for (int i = 0; i < 7; ++i) {                                                          \
      if ((g) < 31) NXT[i] = *(const u32x4*)(np + i * 1024);                                                          \
      __builtin_amdgcn_sched_barrier(6);                                                                     \
      MFV(acc[i], CUR[i], y[g]);                                                                             \
      __builtin_amdgcn_sched_barrier(6);                                                                     \
      const int n = 7 * (g) + i;                                                                             \
      const bool early = n < (5 - UP) * 11;                                                                  \
      const int lc = early ? UP + n / 11 : (ic), j = early ? n % 11 : (ib) + i;                              \
      if (early || (lc >= 5 && lc < 8)) {                                                                    \
        if (j < 7) dma16(l16, wsrc + (j * 33 + 4 * lc + w) * 1024, sbase + (lc % 5) * 28672 + (w * 7 + j) * 1024); \
        else if (j < 11) GLD(y[(4 * lc + j - 7) & 31], l16, ysrc + lc * 4096, ((j - 7) & 3) * 1024);         \
      }                                                                                                      \
      __builtin_amdgcn_sched_barrier(6);                                                                     \
    }                                                                                                        \
  }
#define CHUNK(c, PRE)                                                                                        \
  KSTEP(4 * (c), wc, wn, (c) + 3, 7) KSTEP(4 * (c) + 1, wn, wc, 0, 0) KSTEP(4 * (c) + 2, wc, wn, 0, 0) PRE KSTEP(4 * (c) + 3, wn, wc, (c) + 4, 0)
#define CORR                                                                                                 \
  { asm volatile("s_nop 3" : "+v"(yx));                                                                                                      \
    _Pragma("unroll") for (int i = 0; i < 7; ++i) MFV(acc[i], we[i], yx);                                     \
    __builtin_amdgcn_sched_barrier(6);                                                                       \
  }
  CHUNK(0, CORR WAITB(11 * UP - 1, 1);)
  CHUNK(1, WAITB(22, 2);)
  TS(4, 3);
  CHUNK(2, WAITB(22, 3);)
  CHUNK(3, WAITB(22, 4);)
  TS(4, 4);
  CHUNK(4, WAITB(22, 5);)
  CHUNK(5, WAITB(11, 6);)
  TS(4, 5);
  CHUNK(6, WAITB(0, 7);)
  CHUNK(7, )
  asm volatile("s_nop 15\n\ts_nop 3" : "+v"(acc[0]), "+v"(acc[1]), "+v"(acc[2]), "+v"(acc[3]), "+v"(acc[4]), "+v"(acc[5]), "+v"(acc[6]));
  TS(4, 2);
  char* hp = ws + WS_HHF + ((size_t)(rt * 112 + t0) * 64 + lane) * 16;
#pragma unroll
  for (int i = 0; i < 7; ++i) {
    const f32x16 a = acc[i];
    float hv[8];
#pragma unroll
    for (int j = 0; j < 8; ++j) {
      float m;
      asm("v_max_f32 %0, 0, %1" : "=v"(m) : "v"(a[j]));
      hv[j] = m * r2 * a[j + 8];
    }
    const u32x4 ho = {pack2(hv[0], hv[1]), pack2(hv[2], hv[3]), pack2(hv[4], hv[5]), pack2(hv[6], hv[7])};
    st16(hp + i * 1024, ho);
  }
  TS(4, 6);
}

extern "C" void kernel_launch(void* const* d_in, const int* in_sizes, int n_in, void* d_out, int out_size, void* d_ws,
                              size_t ws_size, hipStream_t stream) {
  const u16* x = (const u16*)d_in[0];
  const u16* wq = (const u16*)d_in[1];
  const u16* wk = (const u16*)d_in[2];
  const u16* wv = (const u16*)d_in[3];
  const u16* wo = (const u16*)d_in[4];
  const u16* wg = (const u16*)d_in[6];
  const u16* wu = (const u16*)d_in[7];
  const u16* wd = (const u16*)d_in[8];
  const u16* cc = (const u16*)d_in[9];
  const u16* wmod = (const u16*)d_in[10];
  char* ws = (char*)d_ws;
    k1_prep<<<232, 256, 0, stream>>>(wq, wk, wv, cc, wmod, ws);
  k2_qkv<<<376, 512, 0, stream>>>(x, wg, wu, ws);
  k3_attn<<<256, 1024, 0, stream>>>(wo, wg, wu, wd, ws);
  k46_gemm<32, K4D, false, K4W><<<256, K4W * 64, 0, stream>>>(x, ws + WS_OF, ws + WS_WOF, ws, (u16*)(ws + WS_X1));
  k5_ffn<<<256, 256, 0, stream>>>(ws);
  k46_gemm<112, K6D, true, K6W><<<256, K6W * 64, 0, stream>>>((const u16*)(ws + WS_X1), ws + WS_HHF, ws + WS_WDF, ws, (u16*)d_out);
}
```

```cpp
#include <hip/hip_runtime.h>
#include <hip/hip_bf16.h>
#include <stdint.h>

#define DEV __device__ __forceinline__
typedef __bf16 bf16x8 __attribute__((ext_vector_type(8)));
typedef __bf16 bf16x2 __attribute__((ext_vector_type(2)));
typedef float f32x16 __attribute__((ext_vector_type(16)));
typedef float f32x4 __attribute__((ext_vector_type(4)));
typedef float f32x2 __attribute__((ext_vector_type(2)));
typedef unsigned u32x4 __attribute__((ext_vector_type(4)));
typedef unsigned u32x2 __attribute__((ext_vector_type(2)));
typedef unsigned short u16;

#define K4W 8
#define K4D 4
#define K6W 4
#define K6D 9
#define K5B 1
#define K6B 2
#define WS_MODP 0x0u
#define WS_MODF 0xC000u
#define WS_STAT 0x10000u
#define WS_WKVF 0x30000u
#define WS_KF 0x40000u
#define WS_VF 0x50000u
#define WS_WQF 0x80000u
#define WS_WOF 0x100000u
#define WS_WGUF 0x200000u
#define WS_WDF 0x600000u
#define WS_QF 0x800000u
#define WS_OF 0xA00000u
#define WS_X1 0xC00000u
#define WS_YF 0xE00000u
#define WS_HHF 0x1000000u

#define TS(k, slot)
DEV unsigned pack2(float a, float b) {
  f32x2 v = {a, b};
  bf16x2 r = __builtin_convertvector(v, bf16x2);
  return __builtin_bit_cast(unsigned, r);
}
DEV float bflo(unsigned u) { return __uint_as_float(u << 16); }
DEV float bfhi(unsigned u) { return __uint_as_float(u & 0xffff0000u); }
DEV f32x16 mfma(u32x4 a, u32x4 b, f32x16 c) {
  return __builtin_amdgcn_mfma_f32_32x32x16_bf16(__builtin_bit_cast(bf16x8, a), __builtin_bit_cast(bf16x8, b), c, 0, 0, 0);
}
template <int CTRL> DEV float dppf(float v) {
  return __builtin_bit_cast(float, __builtin_amdgcn_update_dpp(0, __builtin_bit_cast(int, v), CTRL, 0xf, 0xf, true));
}
DEV float red4(float v) { v += dppf<0xB1>(v); v += dppf<0x4E>(v); return v; }
DEV float red8(float v) { v = red4(v); v += dppf<0x141>(v); return v; }
DEV float red16(float v) { v = red8(v); v += dppf<0x140>(v); return v; }
DEV float rl(float v, int l) { return __builtin_bit_cast(float, __builtin_amdgcn_readlane(__builtin_bit_cast(int, v), l)); }
DEV float wave_sum(float v) { v = red16(v); return (rl(v, 0) + rl(v, 16)) + (rl(v, 32) + rl(v, 48)); }
#define GLD(dst, voff, sptr, imm) \
  asm volatile("global_load_dwordx4 %0, %1, %2 offset:%3" : "=v"(dst) : "v"(voff), "s"(sptr), "n"(imm) : "memory")
#define GLD1(dst, voff, sptr, imm) \
  asm volatile("global_load_dword %0, %1, %2 offset:%3" : "=v"(dst) : "v"(voff), "s"(sptr), "n"(imm) : "memory")
DEV void dma16(unsigned voff, const char* sptr, unsigned ldsoff) {
  asm volatile("s_mov_b32 m0, %2\n\ts_nop 0\n\tglobal_load_lds_dwordx4 %0, %1" ::"v"(voff), "s"(sptr), "s"(ldsoff) : "memory");
}
DEV void st16(void* p, u32x4 v) { asm volatile("global_store_dwordx4 %0, %1, off sc1\n\ts_nop 2" ::"v"(p), "v"(v) : "memory"); }
DEV f32x16 zero16() { f32x16 z; for (int i = 0; i < 16; ++i) z[i] = 0.f; return z; }

extern "C" __global__ __launch_bounds__(256) void k1_prep(const u16* __restrict__ wq, const u16* __restrict__ wk, const u16* __restrict__ wv,
                                               const u16* __restrict__ cc, const u16* __restrict__ wmod, char* __restrict__ ws) {
  __shared__ __attribute__((aligned(16))) u16 tile[64 * 136];
  const int tid = threadIdx.x, lane = tid & 63, w = tid >> 6;
  int b = blockIdx.x;
  TS(0, 0);
  if (b < 8) {
    unsigned long long pc;
    unsigned off, len;
    asm volatile("s_getpc_b64 %0\n.Lpch%=:\n\ts_mov_b32 %1, .Lpch%=-k1_prep\n\ts_mov_b32 %2, k6_down-k1_prep" : "=s"(pc), "=s"(off), "=s"(len));
    const char* base = (const char*)(pc - off);
    u32x4 t[10];
#pragma unroll
    for (int i = 0; i < 10; ++i) { const unsigned o = min((unsigned)(i * 4096 + tid * 16), len + 3072u - 16u); GLD(t[i], o, base, 0); }
    asm volatile("s_waitcnt vmcnt(0)" : "+v"(t[0]), "+v"(t[1]), "+v"(t[2]), "+v"(t[3]), "+v"(t[4]), "+v"(t[5]), "+v"(t[6]), "+v"(t[7]), "+v"(t[8]), "+v"(t[9]));
    return;
  }
  b -= 8;
  if (b < 192) {
    const int cbk = b % 48, kq = b / 48;
    const int c8 = tid & 7, kg = tid >> 3;
    const int k0 = kq * 128 + kg * 4;
    u32x4 wr[4];
#pragma unroll
    for (int i = 0; i < 4; ++i) wr[i] = *(const u32x4*)(wmod + (size_t)(k0 + i) * 3072 + cbk * 64 + c8 * 8);
    u32x2 cr = *(const u32x2*)(cc + k0);
    float cv[4] = {bflo(cr[0]), bfhi(cr[0]), bflo(cr[1]), bfhi(cr[1])};
    float acc[8];
#pragma unroll
    for (int j = 0; j < 8; ++j) acc[j] = 0.f;
#pragma unroll
    for (int i = 0; i < 4; ++i) {
      float s = cv[i] / (1.f + __expf(-cv[i]));
#pragma unroll
      for (int j = 0; j < 4; ++j) { acc[2 * j] += s * bflo(wr[i][j]); acc[2 * j + 1] += s * bfhi(wr[i][j]); }
    }
    float* part = (float*)tile;
#pragma unroll
    for (int j = 0; j < 8; ++j) acc[j] += dppf<0x128>(acc[j]);
    if ((lane & 15) < 8) {
      float* p = part + (w * 4 + (lane >> 4)) * 64 + c8 * 8;
      *(f32x4*)p = f32x4{acc[0], acc[1], acc[2], acc[3]};
      *(f32x4*)(p + 4) = f32x4{acc[4], acc[5], acc[6], acc[7]};
    }
    __syncthreads();
    {
      const int col = tid >> 2, seg = tid & 3;
      float s = 0.f;
#pragma unroll
      for (int i = 0; i < 4; ++i) s += part[(seg * 4 + i) * 64 + col];
      s = red4(s);
      if (seg == 0) ((float*)(ws + WS_MODP))[kq * 3072 + cbk * 64 + col] = s;
    }
    TS(0, 7);
    return;
  }
  b -= 192;
  if (b < 8) {
    const int ks = b * 4 + w, h = lane >> 5, m = lane & 31;
    const u16* src = (m < 16 ? wk : wv) + (m & 15) + (ks * 16 + h * 8) * 16;
    u32x4 o;
#pragma unroll
    for (int j = 0; j < 4; ++j) o[j] = (unsigned)src[(2 * j) * 16] | ((unsigned)src[(2 * j + 1) * 16] << 16);
    *(u32x4*)(ws + WS_WKVF + (ks * 64 + lane) * 16) = o;
    return;
  }
  b -= 8;
  const int kb = b >> 2, nb = b & 3;
  const u16* sl = wq + kb * 64 * 512 + nb * 128;
  u32x4 r[4];
#pragma unroll
  for (int i = 0; i < 4; ++i) {
    const int p = tid + 256 * i, row = p >> 4, c = p & 15;
    r[i] = *(const u32x4*)(sl + row * 512 + c * 8);
  }
#pragma unroll
  for (int i = 0; i < 4; ++i) {
    const int p = tid + 256 * i, row = p >> 4, c = p & 15;
    *(u32x4*)(tile + row * 136 + c * 8) = r[i];
  }
  __syncthreads();
  const int h = lane >> 5, m = lane & 31;
  const int ka = w * 16 + 8 * h, kb2 = ka + 4;
#pragma unroll
  for (int tt = 0; tt < 4; ++tt) {
    const int n = tt * 32 + m;
    const u16* pa = tile + ka * 136 + n;
    const u16* pb = tile + kb2 * 136 + n;
    u32x4 o;
    o[0] = (unsigned)pa[0] | ((unsigned)pa[136] << 16);
    o[1] = (unsigned)pa[272] | ((unsigned)pa[408] << 16);
    o[2] = (unsigned)pb[0] | ((unsigned)pb[136] << 16);
    o[3] = (unsigned)pb[272] | ((unsigned)pb[408] << 16);
    *(u32x4*)(ws + WS_WQF + ((size_t)((nb * 4 + tt) * 32 + kb * 4 + w) * 64 + lane) * 16) = o;
  }
  TS(0, 6);
}

DEV void split_bf(float v, unsigned& hi, unsigned& lo) {
  unsigned p = pack2(v, 0.f) & 0xffffu;
  float r = v - __uint_as_float(p << 16);
  hi = p; lo = pack2(r, 0.f) & 0xffffu;
}

template <bool iskv> DEV void qkv_body(const u16* __restrict__ x, char* __restrict__ ws, char* smem, const int rt, const int cb) {
  const int tid = threadIdx.x, lane = tid & 63;
  const int w = __builtin_amdgcn_readfirstlane(tid >> 6);
  const float* modp = (const float*)(ws + WS_MODP);
  float* sc = (float*)(smem + 33792 + 32768);
  const unsigned l16 = lane * 16;
  u32x4 xr[4];
  {
    const char* xp = (const char*)(x + (size_t)(rt * 32 + w * 4) * 512);
    GLD(xr[0], l16, xp, 0); GLD(xr[1], l16, xp, 1024); GLD(xr[2], l16, xp, 2048); GLD(xr[3], l16, xp, 3072);
  }
  float mp[8];
#pragma unroll
  for (int i = 0; i < 4; ++i) { GLD1(mp[i], tid * 4, modp + i * 3072, 0); GLD1(mp[4 + i], tid * 4, modp + i * 3072, 2048); }
  u32x4 wf[16];
  {
    const char* p = iskv ? ws + WS_WKVF + w * 4096 : ws + WS_WQF + (size_t)((cb * 4 + (w & 3)) * 32 + (w >> 2) * 16) * 1024;
    GLD(wf[0], l16, p, 0); GLD(wf[1], l16, p, 1024); GLD(wf[2], l16, p, 2048); GLD(wf[3], l16, p, 3072);
    if (!iskv) {
#pragma unroll
      for (int i = 1; i < 4; ++i) {
        const char* q = p + i * 4096;
        GLD(wf[4 * i], l16, q, 0); GLD(wf[4 * i + 1], l16, q, 1024); GLD(wf[4 * i + 2], l16, q, 2048); GLD(wf[4 * i + 3], l16, q, 3072);
      }
      asm volatile("s_waitcnt vmcnt(16)" : "+v"(xr[0]), "+v"(xr[1]), "+v"(xr[2]), "+v"(xr[3]), "+v"(mp[0]), "+v"(mp[1]), "+v"(mp[2]),
                   "+v"(mp[3]), "+v"(mp[4]), "+v"(mp[5]), "+v"(mp[6]), "+v"(mp[7]));
    } else {
      asm volatile("s_waitcnt vmcnt(4)" : "+v"(xr[0]), "+v"(xr[1]), "+v"(xr[2]), "+v"(xr[3]), "+v"(mp[0]), "+v"(mp[1]), "+v"(mp[2]),
                   "+v"(mp[3]), "+v"(mp[4]), "+v"(mp[5]), "+v"(mp[6]), "+v"(mp[7]));
    }
  }
  TS(1, 1);
  sc[tid] = (mp[0] + mp[1]) + (mp[2] + mp[3]);
  sc[512 + tid] = 1.f + ((mp[4] + mp[5]) + (mp[6] + mp[7]));
  float v[4][8];
#pragma unroll
  for (int i = 0; i < 4; ++i) {
#pragma unroll
    for (int j = 0; j < 4; ++j) { v[i][2 * j] = bflo(xr[i][j]); v[i][2 * j + 1] = bfhi(xr[i][j]); }
    float s = ((v[i][0] + v[i][1]) + (v[i][2] + v[i][3])) + ((v[i][4] + v[i][5]) + (v[i][6] + v[i][7]));
    const float mean = wave_sum(s) * (1.f / 512.f);
    float q = 0.f;
#pragma unroll
    for (int j = 0; j < 8; ++j) { v[i][j] -= mean; q += v[i][j] * v[i][j]; }
    const float r = 1.f / sqrtf(wave_sum(q) * (1.f / 512.f) + 1e-5f);
#pragma unroll
    for (int j = 0; j < 8; ++j) v[i][j] *= r;
  }
  asm volatile("s_waitcnt lgkmcnt(0)\n\ts_barrier" ::: "memory");
  {
    const f32x4 s0 = *(const f32x4*)(sc + lane * 8), s1 = *(const f32x4*)(sc + lane * 8 + 4);
    const f32x4 c0 = *(const f32x4*)(sc + 512 + lane * 8), c1 = *(const f32x4*)(sc + 512 + lane * 8 + 4);
#pragma unroll
    for (int i = 0; i < 4; ++i) {
      u32x4 o;
      o[0] = pack2(v[i][0] * c0[0] + s0[0], v[i][1] * c0[1] + s0[1]);
      o[1] = pack2(v[i][2] * c0[2] + s0[2], v[i][3] * c0[3] + s0[3]);
      o[2] = pack2(v[i][4] * c1[0] + s1[0], v[i][5] * c1[1] + s1[1]);
      o[3] = pack2(v[i][6] * c1[2] + s1[2], v[i][7] * c1[3] + s1[3]);
      *(u32x4*)(smem + lane * 528 + (w * 4 + i) * 16) = o;
    }
  }
  asm volatile("s_waitcnt vmcnt(0)" : "+v"(wf[0]), "+v"(wf[1]), "+v"(wf[2]), "+v"(wf[3]));
  if (!iskv)
    asm volatile("" : "+v"(wf[4]), "+v"(wf[5]), "+v"(wf[6]), "+v"(wf[7]), "+v"(wf[8]), "+v"(wf[9]), "+v"(wf[10]), "+v"(wf[11]),
                 "+v"(wf[12]), "+v"(wf[13]), "+v"(wf[14]), "+v"(wf[15]));
  if (iskv && rt < 6) {
    const int i = rt * 512 + tid;
    ((float*)(ws + WS_MODF))[i] = (modp[i] + modp[3072 + i]) + (modp[6144 + i] + modp[9216 + i]);
  }
  __syncthreads();
  TS(1, 2);
  const char* hb = smem + (lane >> 5) * 528 + (lane & 31) * 16;
  char* red = smem + 33792;
  f32x16 acc = zero16();
  if (iskv) {
#pragma unroll
    for (int i = 0; i < 4; ++i) acc = mfma(wf[i], *(const u32x4*)(hb + (w * 4 + i) * 1056), acc);
#pragma unroll
    for (int q = 0; q < 4; ++q)
      *(f32x4*)(red + (w * 4 + q) * 1024 + lane * 16) = f32x4{acc[4 * q], acc[4 * q + 1], acc[4 * q + 2], acc[4 * q + 3]};
    __syncthreads();
    if (tid < 256) {
      const int q = w;
      f32x4 s = {0, 0, 0, 0};
#pragma unroll
      for (int p = 0; p < 8; ++p) s += *(const f32x4*)(red + (p * 4 + q) * 1024 + lane * 16);
      const unsigned p0 = pack2(s[0], s[1]), p1 = pack2(s[2], s[3]);
      if (q < 2) {
        *(u32x2*)(ws + WS_KF + rt * 1024 + lane * 16 + q * 8) = u32x2{p0, p1};
      } else {
        const int t = lane & 31, h = lane >> 5;
        const int d0 = 8 * (q - 2) + 4 * h;
        u16* vp = (u16*)(ws + WS_VF + rt * 1024 + (t >> 4) * 512 + ((t >> 2) & 1) * 256 + d0 * 16) + (((t >> 3) & 1) * 4 + (t & 3));
        vp[0] = (u16)p0; vp[8] = (u16)(p0 >> 16); vp[16] = (u16)p1; vp[24] = (u16)(p1 >> 16);
      }
    }
  } else {
    const int kh = w >> 2, ct = w & 3;
#pragma unroll
    for (int i = 0; i < 16; ++i) acc = mfma(wf[i], *(const u32x4*)(hb + (kh * 16 + i) * 1056), acc);
    f32x4 k0, k1, g0, g1;
    if (kh == 0) {
      k0 = f32x4{acc[0], acc[1], acc[2], acc[3]}; k1 = f32x4{acc[4], acc[5], acc[6], acc[7]};
      g0 = f32x4{acc[8], acc[9], acc[10], acc[11]}; g1 = f32x4{acc[12], acc[13], acc[14], acc[15]};
    } else {
      g0 = f32x4{acc[0], acc[1], acc[2], acc[3]}; g1 = f32x4{acc[4], acc[5], acc[6], acc[7]};
      k0 = f32x4{acc[8], acc[9], acc[10], acc[11]}; k1 = f32x4{acc[12], acc[13], acc[14], acc[15]};
    }
    *(f32x4*)(red + w * 2048 + lane * 16) = g0;
    *(f32x4*)(red + w * 2048 + 1024 + lane * 16) = g1;
    __syncthreads();
    k0 += *(const f32x4*)(red + (w ^ 4) * 2048 + lane * 16);
    k1 += *(const f32x4*)(red + (w ^ 4) * 2048 + 1024 + lane * 16);
    const float sc = 0.25f * 1.4426950408889634f;
    k0 *= sc; k1 *= sc;
    u32x4 o = {pack2(k0[0], k0[1]), pack2(k0[2], k0[3]), pack2(k1[0], k1[1]), pack2(k1[2], k1[3])};
    const int head = (cb * 4 + ct) * 2 + kh;
    st16(ws + WS_QF + ((size_t)(rt * 32 + head) * 64 + lane) * 16, o);
  }
}

__global__ __launch_bounds__(512, 4) void k2_qkv(const u16* __restrict__ x, const u16* __restrict__ wg, const u16* __restrict__ wu,
                                                 char* __restrict__ ws) {
  __shared__ __attribute__((aligned(16))) char smem[33792 + 32768 + 4096];
  const int tid = threadIdx.x, lane = tid & 63;
  const int w = __builtin_amdgcn_readfirstlane(tid >> 6);
  const float* modp = (const float*)(ws + WS_MODP);
  float* sc = (float*)(smem + 33792 + 32768);
  int b = blockIdx.x;
  TS(1, 0);
  if (b < 56) {
    const int up = b >= 28, f0 = (b % 28) * 64;
    const int c8 = tid & 7, kg = tid >> 3;
    const u16* src = (up ? wu : wg) + (size_t)(kg * 8) * 1792 + f0 + c8 * 8;
    u32x4 wr[8];
#pragma unroll
    for (int i = 0; i < 8; ++i) wr[i] = *(const u32x4*)(src + i * 1792);
    {
      const float* mp = modp + 1536 + tid;
      sc[tid] = (mp[0] + mp[3072]) + (mp[6144] + mp[9216]);
      sc[512 + tid] = 1.f + ((mp[512] + mp[3584]) + (mp[6656] + mp[9728]));
    }
    __syncthreads();
    const f32x4 s0 = *(const f32x4*)(sc + kg * 8), s1 = *(const f32x4*)(sc + kg * 8 + 4);
    const f32x4 c0 = *(const f32x4*)(sc + 512 + kg * 8), c1 = *(const f32x4*)(sc + 512 + kg * 8 + 4);
    float au[8], ab[8];
#pragma unroll
    for (int j = 0; j < 8; ++j) au[j] = ab[j] = 0.f;
#pragma unroll
    for (int i = 0; i < 8; ++i) {
      const float c = i < 4 ? c0[i & 3] : c1[i & 3], s = i < 4 ? s0[i & 3] : s1[i & 3];
#pragma unroll
      for (int j = 0; j < 4; ++j) {
        const float lo = bflo(wr[i][j]), hi = bfhi(wr[i][j]);
        au[2 * j] += c * lo; au[2 * j + 1] += c * hi; ab[2 * j] += s * lo; ab[2 * j + 1] += s * hi;
      }
    }
#pragma unroll
    for (int j = 0; j < 8; ++j) { au[j] += dppf<0x128>(au[j]); ab[j] += dppf<0x128>(ab[j]); }
    float* part = (float*)smem;
    if ((lane & 15) < 8) {
      float* p = part + (w * 4 + (lane >> 4)) * 128 + c8 * 16;
      *(f32x4*)p = f32x4{au[0], au[1], au[2], au[3]};
      *(f32x4*)(p + 4) = f32x4{au[4], au[5], au[6], au[7]};
      *(f32x4*)(p + 8) = f32x4{ab[0], ab[1], ab[2], ab[3]};
      *(f32x4*)(p + 12) = f32x4{ab[4], ab[5], ab[6], ab[7]};
    }
    __syncthreads();
    const int col = tid >> 3, seg = tid & 7;
    float u = 0.f, bb = 0.f;
#pragma unroll
    for (int i = 0; i < 4; ++i) {
      const float* p = part + (seg * 4 + i) * 128 + (col >> 3) * 16 + (col & 7);
      u += p[0]; bb += p[8];
    }
    u = red8(u); bb = red8(bb);
    if (seg < 2) {
      unsigned uh, ul, bh, bl;
      split_bf(u, uh, ul); split_bf(bb, bh, bl);
      u32x4 o = {uh | (ul << 16), uh | (ul << 16), bh | (bl << 16), bh | (bl << 16)};
      if (seg) o = u32x4{0, 0, 0, 0};
      const int f = f0 + col, t = f >> 4, m = (f & 15) + 16 * up + 32 * seg;
      *(u32x4*)(ws + WS_WGUF + ((size_t)(t * 33 + 32) * 64 + m) * 16) = o;
    }
    TS(1, 7);
    return;
  }
  b -= 56;
  if (b < 64) qkv_body<true>(x, ws, smem, b, 0);
  else qkv_body<false>(x, ws, smem, b & 63, (b >> 6) - 1);
  TS(1, 6);
}

DEV float xhalf_max(float m) { return fmaxf(m, __shfl_xor(m, 32)); }
DEV float max16(const f32x16& s) {
  float a = fmaxf(fmaxf(s[0], s[1]), s[2]), b = fmaxf(fmaxf(s[3], s[4]), s[5]), c = fmaxf(fmaxf(s[6], s[7]), s[8]);
  float d = fmaxf(fmaxf(s[9], s[10]), s[11]), e = fmaxf(fmaxf(s[12], s[13]), s[14]);
  return fmaxf(fmaxf(fmaxf(a, b), c), fmaxf(fmaxf(d, e), s[15]));
}
#define ATT_LOOP \
  "1:\n\tv_exp_f32 v0, v0\n\tv_exp_f32 v1, v1\n\tv_exp_f32 v2, v2\n\t" \
  "v_exp_f32 v3, v3\n\tv_mfma_f32_32x32x16_bf16 v[48:63], v[88:91], v[96:99], v[48:63]\n\ts_waitcnt lgkmcnt(0)\n\tds_read_b128 v[68:71], v100 offset:2048\n\t" \
  "s_mov_b64 exec, %[msk]\n\tds_read_b128 v[84:87], v101 offset:1024\n\tds_read_b128 v[88:91], v101 offset:1536\n\ts_mov_b64 exec, -1\n\t" \
  "v_cvt_pk_bf16_f32 v92, v0, v1\n\tv_exp_f32 v4, v4\n\tv_exp_f32 v5, v5\n\tv_mfma_f32_32x32x16_bf16 v[16:31], v[72:75], v[64:67], v[32:47]\n\t" \
  "v_cvt_pk_bf16_f32 v93, v2, v3\n\tv_exp_f32 v6, v6\n\tv_exp_f32 v7, v7\n\tv_cvt_pk_bf16_f32 v94, v4, v5\n\t" \
  "v_exp_f32 v8, v8\n\tv_exp_f32 v9, v9\n\tv_cvt_pk_bf16_f32 v95, v6, v7\n\tv_exp_f32 v10, v10\n\t" \
  "v_exp_f32 v11, v11\n\tv_mfma_f32_32x32x16_bf16 v[48:63], v[76:79], v[92:95], v[48:63]\n\tv_cvt_pk_bf16_f32 v96, v8, v9\n\tv_exp_f32 v12, v12\n\t" \
  "v_exp_f32 v13, v13\n\tv_cvt_pk_bf16_f32 v97, v10, v11\n\tv_exp_f32 v14, v14\n\tv_exp_f32 v15, v15\n\t" \
  "v_cvt_pk_bf16_f32 v98, v12, v13\n\tv_cvt_pk_bf16_f32 v99, v14, v15\n\tv_exp_f32 v16, v16\n\tv_exp_f32 v17, v17\n\t" \
  "v_exp_f32 v18, v18\n\tv_exp_f32 v19, v19\n\tv_mfma_f32_32x32x16_bf16 v[48:63], v[80:83], v[96:99], v[48:63]\n\ts_waitcnt lgkmcnt(0)\n\t" \
  "ds_read_b128 v[72:75], v100 offset:3072\n\ts_mov_b64 exec, %[msk]\n\tds_read_b128 v[76:79], v101 offset:2048\n\tds_read_b128 v[80:83], v101 offset:2560\n\t" \
  "s_mov_b64 exec, -1\n\tv_cvt_pk_bf16_f32 v92, v16, v17\n\tv_exp_f32 v20, v20\n\tv_exp_f32 v21, v21\n\t" \
  "v_mfma_f32_32x32x16_bf16 v[0:15], v[68:71], v[64:67], v[32:47]\n\tv_cvt_pk_bf16_f32 v93, v18, v19\n\tv_exp_f32 v22, v22\n\tv_exp_f32 v23, v23\n\t" \
  "v_cvt_pk_bf16_f32 v94, v20, v21\n\tv_exp_f32 v24, v24\n\tv_exp_f32 v25, v25\n\tv_cvt_pk_bf16_f32 v95, v22, v23\n\t" \
  "v_exp_f32 v26, v26\n\tv_exp_f32 v27, v27\n\tv_mfma_f32_32x32x16_bf16 v[48:63], v[84:87], v[92:95], v[48:63]\n\tv_add_u32 v100, 0x800, v100\n\t" \
  "v_add_u32 v101, 0x800, v101\n\ts_sub_u32 %[n], %[n], 1\n\ts_cmp_lg_u32 %[n], 0\n\tv_cvt_pk_bf16_f32 v96, v24, v25\n\t" \
  "v_exp_f32 v28, v28\n\tv_exp_f32 v29, v29\n\tv_cvt_pk_bf16_f32 v97, v26, v27\n\tv_exp_f32 v30, v30\n\t" \
  "v_exp_f32 v31, v31\n\tv_cvt_pk_bf16_f32 v98, v28, v29\n\tv_cvt_pk_bf16_f32 v99, v30, v31\n\ts_cbranch_scc1 1b\n\t" \
  "s_nop 1\n\tv_mfma_f32_32x32x16_bf16 v[48:63], v[88:91], v[96:99], v[48:63]\n\ts_nop 7\n\tv_mov_b32 v96, 0\n\t" \
  "v_mov_b32 v97, 0\n\tv_mov_b32 v98, 0\n\tv_mov_b32 v99, 0\n\ts_waitcnt lgkmcnt(0)\n\t" \
  "s_nop 15\n\t\n\t" \
  ""
#define LDK(dst, addr, off) asm volatile("ds_read_b128 %0, %1 offset:" #off : "=v"(dst) : "v"(addr))
#define LDV(d0, d1, addr, off0, off1)                                                                                   \
  asm volatile("s_mov_b64 exec, %3\n\tds_read_b128 %0, %2 offset:" #off0 "\n\tds_read_b128 %1, %2 offset:" #off1        \
               "\n\ts_mov_b64 exec, -1"                                                                                 \
               : "+v"(d0), "+v"(d1)                                                                                     \
               : "v"(addr), "s"(vmask))
#define LWAIT(a, b, c) asm volatile("s_waitcnt lgkmcnt(0)" : "+v"(a), "+v"(b), "+v"(c))

#define LDH(d0, d1, p) \
  asm volatile("global_load_ushort %0, %2, off\n\tglobal_load_ushort %1, %2, off offset:64" : "=&v"(d0), "=&v"(d1) : "v"(p) : "memory")
__global__ __launch_bounds__(1024, 4) void k3_attn(const u16* __restrict__ wo, const u16* __restrict__ wg, const u16* __restrict__ wu,
                                                   const u16* __restrict__ wd, char* __restrict__ ws) {
  __shared__ __attribute__((aligned(16))) char smem[131072 + 4096];
  const int tid = threadIdx.x, lane = tid & 63;
  const int w = __builtin_amdgcn_readfirstlane(tid >> 6);
  const int rt = blockIdx.x & 63, hg = blockIdx.x >> 6;
  TS(2, 0);
  const int kh = w >> 3;
  const unsigned sbase = (unsigned)(uintptr_t)smem;
  const int head = hg * 8 + (w & 7);
  u32x4 q;
  {
    const unsigned l16 = lane * 16;
    GLD(q, l16, ws + WS_QF + (size_t)(rt * 32 + head) * 1024, 0);
    const char* kp = ws + WS_KF + kh * 32768 + (w & 7) * 1024;
    const unsigned lb = sbase + kh * 32768 + (w & 7) * 1024;
#pragma unroll
    for (int i = 0; i < 4; ++i) {
      dma16(l16, kp + i * 8192, lb + i * 8192);
      dma16(l16, kp + i * 8192 + 65536, lb + i * 8192 + 65536);
    }
  }
  unsigned f0[8], f1[8];
  char* fdst;
  unsigned fstep;
  bool fstore;
  {
    int p = blockIdx.x * 12 + (w < 12 ? w : w - 4);
    fstore = w < 12 && p < 2944;
    if (p >= 2944) p -= 128;
    const int h = lane >> 5, m = lane & 31;
    const u16* src;
    int ld, ka, kb2;
    if (p < 1792) {
      const int t = (p >> 6) * 4 + (p & 1), ks = (p >> 1) & 31;
      ld = 1792; ka = 8 * h; kb2 = ka + 4;
      src = (m < 16 ? wg : wu) + ks * 16 * 1792 + t * 16 + (m & 15);
      fdst = ws + WS_WGUF + (size_t)(t * 33 + ks) * 1024; fstep = 2 * 33 * 1024;
    } else {
      const bool isd = p < 2688;
      const int pp = p - (isd ? 1792 : 2688), t = (pp & 7) * 2, ks = pp >> 3, KS = isd ? 112 : 32;
      ld = 512; ka = 4 * h; kb2 = ka + 8;
      src = (isd ? wd : wo) + ks * 16 * 512 + t * 32 + m;
      fdst = ws + (isd ? WS_WDF : WS_WOF) + (size_t)(t * KS + ks) * 1024; fstep = KS * 1024;
    }
    fdst += lane * 16;
    const u16 *pa = src + ka * ld, *pb = src + kb2 * ld;
    const u16* rp[8] = {pa, pa + ld, pa + 2 * ld, pa + 3 * ld, pb, pb + ld, pb + 2 * ld, pb + 3 * ld};
    asm volatile("s_waitcnt vmcnt(6)\n\ts_barrier" : "+v"(q)::"memory");
#pragma unroll
    for (int j = 0; j < 8; ++j) LDH(f0[j], f1[j], rp[j]);
  }
  const unsigned long long vmask = 0x0000ffff0000ffffull;
  const unsigned kbase = sbase + kh * 32768 + lane * 16;
  const unsigned vbase = sbase + 65536 + kh * 32768 + (lane >> 5) * 256 + (lane & 15) * 16;
  const unsigned one1 = (lane & 27) == 16 ? 0x3f803f80u : 0u;
  const u32x4 ones = {one1, one1, one1, one1};
  u32x4 va0 = ones, va1 = ones, vb0 = ones, vb1 = ones, ka, kb;
  TS(2, 1);
  f32x16 o;
  float off;
  for (int attempt = 0; attempt < 2; ++attempt) {
    float m = -3.0e38f;
    const int ntm = attempt ? 32 : 1;
    for (int t = 0; t < ntm; ++t) {
      const unsigned a = kbase + t * 1024;
      LDK(ka, a, 0);
      LWAIT(ka, va0, va1);
      m = fmaxf(m, max16(mfma(ka, q, zero16())));
    }
    m = xhalf_max(m);
    const float thr = attempt ? __builtin_inff() : 1.f;
    off = m + (attempt ? 0.f : 80.f);
    f32x16 c;
#pragma unroll
    for (int i = 0; i < 16; ++i) c[i] = -off;
    o = zero16();
    unsigned kad = kbase, vad = vbase;
    LDK(ka, kad, 0);
    LDK(kb, kad, 1024);
    LDV(va0, va1, vad, 0, 512);
    asm volatile("s_waitcnt lgkmcnt(0)" : "+v"(ka), "+v"(kb), "+v"(va0), "+v"(va1));
    f32x16 s0 = mfma(ka, q, c);
    u32x4 pz = {0, 0, 0, 0};
    for (int seg = 0; seg < 3; ++seg) {
      if (seg == 1 && attempt == 0) asm volatile("s_waitcnt vmcnt(16)\n\ts_barrier" ::: "memory");
      if (seg == 2 && attempt == 0) {
        asm volatile("s_waitcnt vmcnt(0)"
                     : "+v"(f0[0]), "+v"(f0[1]), "+v"(f0[2]), "+v"(f0[3]), "+v"(f0[4]), "+v"(f0[5]), "+v"(f0[6]), "+v"(f0[7]),
                       "+v"(f1[0]), "+v"(f1[1]), "+v"(f1[2]), "+v"(f1[3]), "+v"(f1[4]), "+v"(f1[5]), "+v"(f1[6]), "+v"(f1[7])::"memory");
        if (fstore) {
          const u32x4 g0 = {f0[0] | (f0[1] << 16), f0[2] | (f0[3] << 16), f0[4] | (f0[5] << 16), f0[6] | (f0[7] << 16)};
          const u32x4 g1 = {f1[0] | (f1[1] << 16), f1[2] | (f1[3] << 16), f1[4] | (f1[5] << 16), f1[6] | (f1[7] << 16)};
          asm volatile("global_store_dwordx4 %0, %1, off sc1\n\tglobal_store_dwordx4 %2, %3, off sc1\n\ts_nop 1"
                       ::"v"(fdst), "v"(g0), "v"(fdst + fstep), "v"(g1) : "memory");
        }
      }
      if (seg) {
        if (__any(o[8] > thr)) {
          const int e = __builtin_amdgcn_frexp_expf(o[8]) + 80;
          const float f = (float)e;
#pragma unroll
          for (int i = 0; i < 9; ++i) o[i] = __builtin_amdgcn_ldexpf(o[i], -e);
          off += f;
#pragma unroll
          for (int i = 0; i < 16; ++i) { c[i] = -off; s0[i] -= f; }
        }
      }
      int np = seg == 0 ? 2 : seg == 1 ? 5 : 9;
      asm volatile("s_nop 15\n\t" ATT_LOOP
                   : "+{v[0:15]}"(s0), "+{v[48:63]}"(o), "+{v[72:75]}"(kb), "+{v[76:79]}"(va0), "+{v[80:83]}"(va1),
                     "+{v[84:87]}"(vb0), "+{v[88:91]}"(vb1), "+{v[96:99]}"(pz), "+{v100}"(kad), "+{v101}"(vad), [n] "+s"(np)
                   : "{v[32:47]}"(c), "{v[64:67]}"(q), [msk] "s"(vmask)
                   : "v16", "v17", "v18", "v19", "v20", "v21", "v22", "v23", "v24", "v25", "v26", "v27", "v28", "v29", "v30", "v31",
                     "v68", "v69", "v70", "v71", "v92", "v93", "v94", "v95", "scc", "memory");
    }
    if (attempt) break;
    float a = 0.f, s = o[8];
#pragma unroll
    for (int i = 0; i < 8; ++i) { a = fmaxf(a, fabsf(o[i])); s += o[i] * 0.f; }
    const bool ok = (s < 3.0e38f) && (a >= 7.8886091e-31f);
    if (!__any(!ok)) break;
  }
  TS(2, 2);
  __syncthreads();
  char* ex = smem + (w & 7) * 3072 + lane * 16;
  if (kh) {
    *(f32x4*)(ex) = f32x4{o[0], o[1], o[2], o[3]};
    *(f32x4*)(ex + 1024) = f32x4{o[4], o[5], o[6], o[7]};
    *(f32x2*)(ex + 2048) = f32x2{o[8], off};
  }
  __syncthreads();
  if (!kh) {
    const f32x4 g0 = *(const f32x4*)(ex), g1 = *(const f32x4*)(ex + 1024);
    const f32x2 g2 = *(const f32x2*)(ex + 2048);
    const float m = fmaxf(off, g2[1]);
    float fk = __builtin_amdgcn_exp2f(off - m), fg = __builtin_amdgcn_exp2f(g2[1] - m);
    const float inv = 1.f / (o[8] * fk + g2[0] * fg);
    fk *= inv; fg *= inv;
    const f32x4 k0 = f32x4{o[0], o[1], o[2], o[3]} * fk + g0 * fg, k1 = f32x4{o[4], o[5], o[6], o[7]} * fk + g1 * fg;
    const u32x4 ov = {pack2(k0[0], k0[1]), pack2(k0[2], k0[3]), pack2(k1[0], k1[1]), pack2(k1[2], k1[3])};
    st16(ws + WS_OF + ((size_t)(rt * 32 + head) * 64 + lane) * 16, ov);
  }
  TS(2, 6);
}

template <int KS, int DEPTH, bool FINAL, int NW>
DEV void k46_gemm(const u16* __restrict__ res, const char* __restrict__ af, const char* __restrict__ wf,
                                                   char* __restrict__ ws, u16* __restrict__ out) {
  __shared__ __attribute__((aligned(16))) float red[NW * 64 * 68];
  const int tid = threadIdx.x, lane = tid & 63;
  const int w = __builtin_amdgcn_readfirstlane(tid >> 6);
  const int b = blockIdx.x;
  const int rb = (b & 7) * 4 + ((b >> 3) & 3), cbk = b >> 5;
  constexpr int N = KS / NW, E = 8 / NW;
  TS(FINAL ? 5 : 3, 0);
  const unsigned l16 = lane * 16;
  const char* ap = af + (size_t)(rb * 2 * KS + w * N) * 1024;
  const char* wp = wf + (size_t)(cbk * 2 * KS + w * N) * 1024;
  u32x4 fa[DEPTH][2], fw[DEPTH][2];
#define LD4(s, k)                                                                                     \
  {                                                                                                   \
    GLD(fa[s][0], l16, ap + ((k) >> 2) * 4096, ((k) & 3) * 1024);                                     \
    GLD(fw[s][0], l16, wp + ((k) >> 2) * 4096, ((k) & 3) * 1024);                                     \
    GLD(fa[s][1], l16, ap + KS * 1024 + ((k) >> 2) * 4096, ((k) & 3) * 1024);                         \
    GLD(fw[s][1], l16, wp + KS * 1024 + ((k) >> 2) * 4096, ((k) & 3) * 1024);                         \
  }
#pragma unroll
  for (int i = 0; i < DEPTH; ++i) LD4(i, i)
  const int cg = tid & 7, r0 = tid >> 3;
  const int col = cbk * 64 + cg * 8;
  const float* modf = (const float*)(ws + WS_MODF);
  u32x4 xr[2], g0u, g1u, c0u, c1u;
  {
    const char* rp = (const char*)(res + (size_t)(rb * 64) * 512 + cbk * 64);
    const unsigned ro = r0 * 1024 + cg * 16;
    GLD(xr[0], ro, rp, 0);
    if (E == 2) { GLD(xr[1], ro, rp + 32768, 0); } else xr[1] = u32x4{0, 0, 0, 0};
    const unsigned mo = cg * 32;
    const char* gp = (const char*)(modf + (FINAL ? 2560 : 1024) + cbk * 64);
    GLD(g0u, mo, gp, 0); GLD(g1u, mo, gp, 16);
    const char* cp = (const char*)(modf + 2048 + cbk * 64);
    GLD(c0u, mo, cp, 0); GLD(c1u, mo, cp, 16);
  }
  f32x16 acc[2][2];
  acc[0][0] = acc[0][1] = acc[1][0] = acc[1][1] = zero16();
#pragma unroll
  for (int k = 0; k < N; ++k) {
    const int s = k % DEPTH;
    const int after = 4 * ((k + DEPTH < N ? k + DEPTH : N) - k - 1) + (k < DEPTH ? 4 + E : 0);
    asm volatile("s_waitcnt vmcnt(%4)" : "+v"(fa[s][0]), "+v"(fa[s][1]), "+v"(fw[s][0]), "+v"(fw[s][1]) : "n"(after));
    if (k == 0) TS(FINAL ? 5 : 3, 1);
    acc[0][0] = mfma(fw[s][0], fa[s][0], acc[0][0]);
    acc[0][1] = mfma(fw[s][0], fa[s][1], acc[0][1]);
    acc[1][0] = mfma(fw[s][1], fa[s][0], acc[1][0]);
    acc[1][1] = mfma(fw[s][1], fa[s][1], acc[1][1]);
    if (k + DEPTH < N) LD4(s, k + DEPTH)
  }
  asm volatile("s_waitcnt vmcnt(0)" : "+v"(xr[0]), "+v"(xr[1]), "+v"(g0u), "+v"(g1u), "+v"(c0u), "+v"(c1u));
  TS(FINAL ? 5 : 3, 2);
  const f32x4 g0 = __builtin_bit_cast(f32x4, g0u), g1 = __builtin_bit_cast(f32x4, g1u);
  const f32x4 c0 = __builtin_bit_cast(f32x4, c0u) + 1.f, c1 = __builtin_bit_cast(f32x4, c1u) + 1.f;
  {
    float* rp = red + w * (64 * 68) + (lane & 31) * 68 + (lane >> 5) * 4;
#pragma unroll
    for (int i = 0; i < 2; ++i)
#pragma unroll
      for (int j = 0; j < 2; ++j)
#pragma unroll
        for (int q = 0; q < 4; ++q)
          *(f32x4*)(rp + j * 32 * 68 + i * 32 + q * 8) =
              f32x4{acc[i][j][4 * q], acc[i][j][4 * q + 1], acc[i][j][4 * q + 2], acc[i][j][4 * q + 3]};
  }
  __syncthreads();
#pragma unroll
  for (int e = 0; e < E; ++e) {
    const int r = r0 + 32 * e, row = rb * 64 + r;
    f32x4 s0 = {0, 0, 0, 0}, s1 = s0;
#pragma unroll
    for (int p = 0; p < NW; ++p) {
      const float* rp = red + p * (64 * 68) + r * 68 + cg * 8;
      s0 += *(const f32x4*)rp; s1 += *(const f32x4*)(rp + 4);
    }
    const f32x4 x0 = {bflo(xr[e][0]), bfhi(xr[e][0]), bflo(xr[e][1]), bfhi(xr[e][1])};
    const f32x4 x1 = {bflo(xr[e][2]), bfhi(xr[e][2]), bflo(xr[e][3]), bfhi(xr[e][3])};
    s0 = x0 + g0 * s0; s1 = x1 + g1 * s1;
    const u32x4 o = {pack2(s0[0], s0[1]), pack2(s0[2], s0[3]), pack2(s1[0], s1[1]), pack2(s1[2], s1[3])};
    if (FINAL) *(u32x4*)(out + (size_t)row * 512 + col) = o; else st16(out + (size_t)row * 512 + col, o);
    if (!FINAL) {
      const f32x4 y0 = s0 * c0, y1 = s1 * c1;
      const u32x4 y = {pack2(y0[0], y0[1]), pack2(y0[2], y0[3]), pack2(y1[0], y1[1]), pack2(y1[2], y1[3])};
      st16(ws + WS_YF + (size_t)((row >> 5) * 32 + (col >> 4)) * 1024 + ((col >> 3) & 1) * 512 + (row & 31) * 16, y);
      const float mean = red8(((s0[0] + s0[1]) + (s0[2] + s0[3])) + ((s1[0] + s1[1]) + (s1[2] + s1[3]))) * (1.f / 64.f);
      s0 -= mean; s1 -= mean;
      s0 *= s0; s1 *= s1;
      const float m2 = red8(((s0[0] + s0[1]) + (s0[2] + s0[3])) + ((s1[0] + s1[1]) + (s1[2] + s1[3])));
      if (cg == 0) *(f32x2*)(ws + WS_STAT + (size_t)(row * 8 + cbk) * 8) = f32x2{mean, m2};
    }
  }
  TS(FINAL ? 5 : 3, 6);
}

extern "C" __global__ __launch_bounds__(K4W * 64, 2) void k4_oproj(const u16* __restrict__ res, char* __restrict__ ws) {
  k46_gemm<32, K4D, false, K4W>(res, ws + WS_OF, ws + WS_WOF, ws, (u16*)(ws + WS_X1));
}

__global__ __launch_bounds__(256, K5B) void k5_ffn(char* __restrict__ ws) {
  __shared__ __attribute__((aligned(16))) char smem[5 * 28672 + 7168];
  const int tid = threadIdx.x, lane = tid & 63;
  const int w = __builtin_amdgcn_readfirstlane(tid >> 6);
  const int b = blockIdx.x, xc = b & 7, idx = b >> 3;
  const int rb = (xc & 1) * 8 + (idx & 7), cbk = (xc >> 1) * 4 + (idx >> 3);
  const int rt = rb * 4 + w, t0 = cbk * 7;
  TS(4, 0);
  const unsigned sbase = (unsigned)(uintptr_t)smem;
  const unsigned l16 = lane * 16;
  const char* wsrc = ws + WS_WGUF + (size_t)t0 * 33 * 1024;
  const char* ysrc = ws + WS_YF + (size_t)rt * 32 * 1024;
  u32x4 st[4], y[32];
  {
    const char* sp = ws + WS_STAT + (size_t)(rt * 32) * 64;
    const unsigned so = (lane & 31) * 64;
    GLD(st[0], so, sp, 0); GLD(st[1], so, sp, 16); GLD(st[2], so, sp, 32); GLD(st[3], so, sp, 48);
    const int i2 = w + 4 < 7 ? w + 4 : 6;
    dma16(l16, wsrc + (w * 33 + 32) * 1024, sbase + 5 * 28672 + w * 1024);
    dma16(l16, wsrc + (i2 * 33 + 32) * 1024, sbase + 5 * 28672 + i2 * 1024);
  }
#define ISSUE(c)                                                                                             \
  {                                                                                                          \
    _Pragma("unroll") for (int i = 0; i < 7; ++i)                                                            \
        dma16(l16, wsrc + (i * 33 + 4 * (c) + w) * 1024, sbase + ((c) % 5) * 28672 + (w * 7 + i) * 1024);    \
    const char* yp = ysrc + (c) * 4096;                                                                      \
    GLD(y[4 * (c)], l16, yp, 0); GLD(y[4 * (c) + 1], l16, yp, 1024);                                         \
    GLD(y[4 * (c) + 2], l16, yp, 2048); GLD(y[4 * (c) + 3], l16, yp, 3072);                                  \
  }
#define WAITB(n, c)                                                                                          \
  asm volatile("s_waitcnt vmcnt(%4)\n\ts_barrier"                                                            \
               : "+v"(y[4 * (c)]), "+v"(y[4 * (c) + 1]), "+v"(y[4 * (c) + 2]), "+v"(y[4 * (c) + 3]) : "n"(n) : "memory")
#define UP 2
  ISSUE(0) ISSUE(1)
  if (UP > 2) ISSUE(2)
  f32x16 acc[7];
#pragma unroll
  for (int i = 0; i < 7; ++i) { acc[i] = zero16(); asm volatile("" : "+v"(acc[i])); }
  const char* lp = smem + lane * 16;
  u32x4 wc[7], wn[7];
  WAITB(11 * (UP - 1), 0);
  asm volatile("" : "+v"(st[0]), "+v"(st[1]), "+v"(st[2]), "+v"(st[3]));
#pragma unroll
  for (int i = 0; i < 7; ++i) wc[i] = *(const u32x4*)(lp + i * 1024);
  u32x4 we[7];
#pragma unroll
  for (int i = 0; i < 7; ++i) we[i] = *(const u32x4*)(lp + 5 * 28672 + i * 1024);
  TS(4, 1);
  float r2;
  u32x4 yx;
  {
    float mb[8], m2 = 0.f, mean = 0.f;
#pragma unroll
    for (int i = 0; i < 4; ++i) {
      mb[2 * i] = __uint_as_float(st[i][0]); mb[2 * i + 1] = __uint_as_float(st[i][2]);
      m2 += __uint_as_float(st[i][1]) + __uint_as_float(st[i][3]);
      mean += mb[2 * i] + mb[2 * i + 1];
    }
    mean *= 0.125f;
    float d = 0.f;
#pragma unroll
    for (int i = 0; i < 8; ++i) d += (mb[i] - mean) * (mb[i] - mean);
    const float var = (m2 + 64.f * d) * (1.f / 512.f) + 1e-5f;
    r2 = 1.f / var;
    unsigned mh, ml, rh, rlo;
    split_bf(-mean, mh, ml); split_bf(sqrtf(var), rh, rlo);
    yx = u32x4{mh | (mh << 16), ml | (ml << 16), rh | (rh << 16), rlo | (rlo << 16)};
    if (lane >= 32) yx = u32x4{0, 0, 0, 0};
  }
#define MFV(c, a, b) asm volatile("v_mfma_f32_32x32x16_bf16 %0, %1, %2, %0" : "+v"(c) : "v"(a), "v"(b))
#define KSTEP(g, CUR, NXT, ic, ib)                                                                                   \
  {                                                                                                          \
    const char* np = lp + ((((g) + 1) / 4) % 5) * 28672 + (((g) + 1) % 4) * 7168;                            \
    _Pragma("unroll") for (int i = 0; i < 7; ++i) {                                                          \
      if ((g) < 31) NXT[i] = *(const u32x4*)(np + i * 1024);                                                          \
      __builtin_amdgcn_sched_barrier(6);                                                                     \
      MFV(acc[i], CUR[i], y[g]);                                                                             \
      __builtin_amdgcn_sched_barrier(6);                                                                     \
      const int n = 7 * (g) + i;                                                                             \
      const bool early = n < (5 - UP) * 11;                                                                  \
      const int lc = early ? UP + n / 11 : (ic), j = early ? n % 11 : (ib) + i;                              \
      if (early || (lc >= 5 && lc < 8)) {                                                                    \
        if (j < 7) dma16(l16, wsrc + (j * 33 + 4 * lc + w) * 1024, sbase + (lc % 5) * 28672 + (w * 7 + j) * 1024); \
        else if (j < 11) GLD(y[(4 * lc + j - 7) & 31], l16, ysrc + lc * 4096, ((j - 7) & 3) * 1024);         \
      }                                                                                                      \
      __builtin_amdgcn_sched_barrier(6);                                                                     \
    }                                                                                                        \
  }
#define CHUNK(c, PRE)                                                                                        \
  KSTEP(4 * (c), wc, wn, (c) + 3, 7) KSTEP(4 * (c) + 1, wn, wc, 0, 0) KSTEP(4 * (c) + 2, wc, wn, 0, 0) PRE KSTEP(4 * (c) + 3, wn, wc, (c) + 4, 0)
#define CORR                                                                                                 \
  { asm volatile("s_nop 3" : "+v"(yx));                                                                                                      \
    _Pragma("unroll") for (int i = 0; i < 7; ++i) MFV(acc[i], we[i], yx);                                     \
    __builtin_amdgcn_sched_barrier(6);                                                                       \
  }
  CHUNK(0, CORR WAITB(11 * UP - 1, 1);)
  CHUNK(1, WAITB(22, 2);)
  TS(4, 3);
  CHUNK(2, WAITB(22, 3);)
  CHUNK(3, WAITB(22, 4);)
  TS(4, 4);
  CHUNK(4, WAITB(22, 5);)
  CHUNK(5, WAITB(11, 6);)
  TS(4, 5);
  CHUNK(6, WAITB(0, 7);)
  CHUNK(7, )
  asm volatile("s_nop 15\n\ts_nop 3" : "+v"(acc[0]), "+v"(acc[1]), "+v"(acc[2]), "+v"(acc[3]), "+v"(acc[4]), "+v"(acc[5]), "+v"(acc[6]));
  TS(4, 2);
  char* hp = ws + WS_HHF + ((size_t)(rt * 112 + t0) * 64 + lane) * 16;
#pragma unroll
  for (int i = 0; i < 7; ++i) {
    const f32x16 a = acc[i];
    float hv[8];
#pragma unroll
    for (int j = 0; j < 8; ++j) {
      float m;
      asm("v_max_f32 %0, 0, %1" : "=v"(m) : "v"(a[j]));
      hv[j] = m * r2 * a[j + 8];
    }
    const u32x4 ho = {pack2(hv[0], hv[1]), pack2(hv[2], hv[3]), pack2(hv[4], hv[5]), pack2(hv[6], hv[7])};
    st16(hp + i * 1024, ho);
  }
  TS(4, 6);
}

extern "C" __global__ __launch_bounds__(K6W * 64, 2) void k6_down(char* __restrict__ ws, u16* __restrict__ out) {
  k46_gemm<112, K6D, true, K6W>((const u16*)(ws + WS_X1), ws + WS_HHF, ws + WS_WDF, ws, out);
}

extern "C" void kernel_launch(void* const* d_in, const int* in_sizes, int n_in, void* d_out, int out_size, void* d_ws,
                              size_t ws_size, hipStream_t stream) {
  const u16* x = (const u16*)d_in[0];
  const u16* wq = (const u16*)d_in[1];
  const u16* wk = (const u16*)d_in[2];
  const u16* wv = (const u16*)d_in[3];
  const u16* wo = (const u16*)d_in[4];
  const u16* wg = (const u16*)d_in[6];
  const u16* wu = (const u16*)d_in[7];
  const u16* wd = (const u16*)d_in[8];
  const u16* cc = (const u16*)d_in[9];
  const u16* wmod = (const u16*)d_in[10];
  char* ws = (char*)d_ws;
    k1_prep<<<240, 256, 0, stream>>>(wq, wk, wv, cc, wmod, ws);
  k2_qkv<<<376, 512, 0, stream>>>(x, wg, wu, ws);
  k3_attn<<<256, 1024, 0, stream>>>(wo, wg, wu, wd, ws);
  k4_oproj<<<256, K4W * 64, 0, stream>>>(x, ws);
  k5_ffn<<<256, 256, 0, stream>>>(ws);
  k6_down<<<256, K6W * 64, 0, stream>>>(ws, (u16*)d_out);
}
```

```cpp
#include <hip/hip_runtime.h>
#include <hip/hip_bf16.h>
#include <stdint.h>

#define DEV __device__ __forceinline__
typedef __bf16 bf16x8 __attribute__((ext_vector_type(8)));
typedef __bf16 bf16x2 __attribute__((ext_vector_type(2)));
typedef float f32x16 __attribute__((ext_vector_type(16)));
typedef float f32x4 __attribute__((ext_vector_type(4)));
typedef float f32x2 __attribute__((ext_vector_type(2)));
typedef unsigned u32x4 __attribute__((ext_vector_type(4)));
typedef unsigned u32x2 __attribute__((ext_vector_type(2)));
typedef unsigned short u16;

#define K4W 8
#define K4D 4
#define K6W 4
#define K6D 9
#define K5B 1
#define K6B 2
#define WS_MODP 0x0u
#define WS_MODF 0xC000u
#define WS_STAT 0x10000u
#define WS_WKVF 0x30000u
#define WS_KF 0x40000u
#define WS_VF 0x50000u
#define WS_WQF 0x80000u
#define WS_WOF 0x100000u
#define WS_WGUF 0x200000u
#define WS_WDF 0x600000u
#define WS_QF 0x800000u
#define WS_OF 0xA00000u
#define WS_X1 0xC00000u
#define WS_YF 0xE00000u
#define WS_HHF 0x1000000u

#define TS(k, slot)
DEV unsigned pack2(float a, float b) {
  f32x2 v = {a, b};
  bf16x2 r = __builtin_convertvector(v, bf16x2);
  return __builtin_bit_cast(unsigned, r);
}
DEV float bflo(unsigned u) { return __uint_as_float(u << 16); }
DEV float bfhi(unsigned u) { return __uint_as_float(u & 0xffff0000u); }
DEV f32x16 mfma(u32x4 a, u32x4 b, f32x16 c) {
  return __builtin_amdgcn_mfma_f32_32x32x16_bf16(__builtin_bit_cast(bf16x8, a), __builtin_bit_cast(bf16x8, b), c, 0, 0, 0);
}
template <int CTRL> DEV float dppf(float v) {
  return __builtin_bit_cast(float, __builtin_amdgcn_update_dpp(0, __builtin_bit_cast(int, v), CTRL, 0xf, 0xf, true));
}
DEV float red4(float v) { v += dppf<0xB1>(v); v += dppf<0x4E>(v); return v; }
DEV float red8(float v) { v = red4(v); v += dppf<0x141>(v); return v; }
DEV float red16(float v) { v = red8(v); v += dppf<0x140>(v); return v; }
DEV float rl(float v, int l) { return __builtin_bit_cast(float, __builtin_amdgcn_readlane(__builtin_bit_cast(int, v), l)); }
DEV float wave_sum(float v) { v = red16(v); return (rl(v, 0) + rl(v, 16)) + (rl(v, 32) + rl(v, 48)); }
#define GLD(dst, voff, sptr, imm) \
  asm volatile("global_load_dwordx4 %0, %1, %2 offset:%3" : "=v"(dst) : "v"(voff), "s"(sptr), "n"(imm) : "memory")
#define GLD1(dst, voff, sptr, imm) \
  asm volatile("global_load_dword %0, %1, %2 offset:%3" : "=v"(dst) : "v"(voff), "s"(sptr), "n"(imm) : "memory")
DEV void dma16(unsigned voff, const char* sptr, unsigned ldsoff) {
  asm volatile("s_mov_b32 m0, %2\n\ts_nop 0\n\tglobal_load_lds_dwordx4 %0, %1" ::"v"(voff), "s"(sptr), "s"(ldsoff) : "memory");
}
DEV void st16(void* p, u32x4 v) { asm volatile("global_store_dwordx4 %0, %1, off sc1\n\ts_nop 2" ::"v"(p), "v"(v) : "memory"); }
DEV f32x16 zero16() { f32x16 z; for (int i = 0; i < 16; ++i) z[i] = 0.f; return z; }

__global__ __launch_bounds__(256) void k1_prep(const u16* __restrict__ wq, const u16* __restrict__ wk, const u16* __restrict__ wv,
                                               const u16* __restrict__ cc, const u16* __restrict__ wmod, char* __restrict__ ws,
                                               const u16* __restrict__ x) {
  __shared__ __attribute__((aligned(16))) u16 tile[64 * 136];
  const int tid = threadIdx.x, lane = tid & 63, w = tid >> 6;
  int b = blockIdx.x;
  TS(0, 0);
  if (b >= 232) {
    const char* base = (const char*)x + (size_t)(b - 232) * 32768;
    u32x4 t[8];
#pragma unroll
    for (int i = 0; i < 8; ++i) GLD(t[i], tid * 16, base + i * 4096, 0);
    asm volatile("s_waitcnt vmcnt(0)" : "+v"(t[0]), "+v"(t[1]), "+v"(t[2]), "+v"(t[3]), "+v"(t[4]), "+v"(t[5]), "+v"(t[6]), "+v"(t[7]));
    return;
  }
  if (b < 192) {
    const int cbk = b % 48, kq = b / 48;
    const int c8 = tid & 7, kg = tid >> 3;
    const int k0 = kq * 128 + kg * 4;
    u32x4 wr[4];
#pragma unroll
    for (int i = 0; i < 4; ++i) wr[i] = *(const u32x4*)(wmod + (size_t)(k0 + i) * 3072 + cbk * 64 + c8 * 8);
    u32x2 cr = *(const u32x2*)(cc + k0);
    float cv[4] = {bflo(cr[0]), bfhi(cr[0]), bflo(cr[1]), bfhi(cr[1])};
    float acc[8];
#pragma unroll
    for (int j = 0; j < 8; ++j) acc[j] = 0.f;
#pragma unroll
    for (int i = 0; i < 4; ++i) {
      float s = cv[i] / (1.f + __expf(-cv[i]));
#pragma unroll
      for (int j = 0; j < 4; ++j) { acc[2 * j] += s * bflo(wr[i][j]); acc[2 * j + 1] += s * bfhi(wr[i][j]); }
    }
    float* part = (float*)tile;
#pragma unroll
    for (int j = 0; j < 8; ++j) acc[j] += dppf<0x128>(acc[j]);
    if ((lane & 15) < 8) {
      float* p = part + (w * 4 + (lane >> 4)) * 64 + c8 * 8;
      *(f32x4*)p = f32x4{acc[0], acc[1], acc[2], acc[3]};
      *(f32x4*)(p + 4) = f32x4{acc[4], acc[5], acc[6], acc[7]};
    }
    __syncthreads();
    {
      const int col = tid >> 2, seg = tid & 3;
      float s = 0.f;
#pragma unroll
      for (int i = 0; i < 4; ++i) s += part[(seg * 4 + i) * 64 + col];
      s = red4(s);
      if (seg == 0) ((float*)(ws + WS_MODP))[kq * 3072 + cbk * 64 + col] = s;
    }
    TS(0, 7);
    return;
  }
  b -= 192;
  if (b < 8) {
    const int ks = b * 4 + w, h = lane >> 5, m = lane & 31;
    const u16* src = (m < 16 ? wk : wv) + (m & 15) + (ks * 16 + h * 8) * 16;
    u32x4 o;
#pragma unroll
    for (int j = 0; j < 4; ++j) o[j] = (unsigned)src[(2 * j) * 16] | ((unsigned)src[(2 * j + 1) * 16] << 16);
    *(u32x4*)(ws + WS_WKVF + (ks * 64 + lane) * 16) = o;
    return;
  }
  b -= 8;
  const int kb = b >> 2, nb = b & 3;
  const u16* sl = wq + kb * 64 * 512 + nb * 128;
  u32x4 r[4];
#pragma unroll
  for (int i = 0; i < 4; ++i) {
    const int p = tid + 256 * i, row = p >> 4, c = p & 15;
    r[i] = *(const u32x4*)(sl + row * 512 + c * 8);
  }
#pragma unroll
  for (int i = 0; i < 4; ++i) {
    const int p = tid + 256 * i, row = p >> 4, c = p & 15;
    *(u32x4*)(tile + row * 136 + c * 8) = r[i];
  }
  __syncthreads();
  const int h = lane >> 5, m = lane & 31;
  const int ka = w * 16 + 8 * h, kb2 = ka + 4;
#pragma unroll
  for (int tt = 0; tt < 4; ++tt) {
    const int n = tt * 32 + m;
    const u16* pa = tile + ka * 136 + n;
    const u16* pb = tile + kb2 * 136 + n;
    u32x4 o;
    o[0] = (unsigned)pa[0] | ((unsigned)pa[136] << 16);
    o[1] = (unsigned)pa[272] | ((unsigned)pa[408] << 16);
    o[2] = (unsigned)pb[0] | ((unsigned)pb[136] << 16);
    o[3] = (unsigned)pb[272] | ((unsigned)pb[408] << 16);
    *(u32x4*)(ws + WS_WQF + ((size_t)((nb * 4 + tt) * 32 + kb * 4 + w) * 64 + lane) * 16) = o;
  }
  TS(0, 6);
}

DEV void split_bf(float v, unsigned& hi, unsigned& lo) {
  unsigned p = pack2(v, 0.f) & 0xffffu;
  float r = v - __uint_as_float(p << 16);
  hi = p; lo = pack2(r, 0.f) & 0xffffu;
}

template <bool iskv> DEV void qkv_body(const u16* __restrict__ x, char* __restrict__ ws, char* smem, const int rt, const int cb) {
  const int tid = threadIdx.x, lane = tid & 63;
  const int w = __builtin_amdgcn_readfirstlane(tid >> 6);
  const float* modp = (const float*)(ws + WS_MODP);
  float* sc = (float*)(smem + 33792 + 32768);
  const unsigned l16 = lane * 16;
  u32x4 xr[4];
  {
    const char* xp = (const char*)(x + (size_t)(rt * 32 + w * 4) * 512);
    GLD(xr[0], l16, xp, 0); GLD(xr[1], l16, xp, 1024); GLD(xr[2], l16, xp, 2048); GLD(xr[3], l16, xp, 3072);
  }
  float mp[8];
#pragma unroll
  for (int i = 0; i < 4; ++i) { GLD1(mp[i], tid * 4, modp + i * 3072, 0); GLD1(mp[4 + i], tid * 4, modp + i * 3072, 2048); }
  u32x4 wf[16];
  {
    const char* p = iskv ? ws + WS_WKVF + w * 4096 : ws + WS_WQF + (size_t)((cb * 4 + (w & 3)) * 32 + (w >> 2) * 16) * 1024;
    GLD(wf[0], l16, p, 0); GLD(wf[1], l16, p, 1024); GLD(wf[2], l16, p, 2048); GLD(wf[3], l16, p, 3072);
    if (!iskv) {
#pragma unroll
      for (int i = 1; i < 4; ++i) {
        const char* q = p + i * 4096;
        GLD(wf[4 * i], l16, q, 0); GLD(wf[4 * i + 1], l16, q, 1024); GLD(wf[4 * i + 2], l16, q, 2048); GLD(wf[4 * i + 3], l16, q, 3072);
      }
      asm volatile("s_waitcnt vmcnt(16)" : "+v"(xr[0]), "+v"(xr[1]), "+v"(xr[2]), "+v"(xr[3]), "+v"(mp[0]), "+v"(mp[1]), "+v"(mp[2]),
                   "+v"(mp[3]), "+v"(mp[4]), "+v"(mp[5]), "+v"(mp[6]), "+v"(mp[7]));
    } else {
      asm volatile("s_waitcnt vmcnt(4)" : "+v"(xr[0]), "+v"(xr[1]), "+v"(xr[2]), "+v"(xr[3]), "+v"(mp[0]), "+v"(mp[1]), "+v"(mp[2]),
                   "+v"(mp[3]), "+v"(mp[4]), "+v"(mp[5]), "+v"(mp[6]), "+v"(mp[7]));
    }
  }
  TS(1, 1);
  sc[tid] = (mp[0] + mp[1]) + (mp[2] + mp[3]);
  sc[512 + tid] = 1.f + ((mp[4] + mp[5]) + (mp[6] + mp[7]));
  float v[4][8];
#pragma unroll
  for (int i = 0; i < 4; ++i) {
#pragma unroll
    for (int j = 0; j < 4; ++j) { v[i][2 * j] = bflo(xr[i][j]); v[i][2 * j + 1] = bfhi(xr[i][j]); }
    float s = ((v[i][0] + v[i][1]) + (v[i][2] + v[i][3])) + ((v[i][4] + v[i][5]) + (v[i][6] + v[i][7]));
    const float mean = wave_sum(s) * (1.f / 512.f);
    float q = 0.f;
#pragma unroll
    for (int j = 0; j < 8; ++j) { v[i][j] -= mean; q += v[i][j] * v[i][j]; }
    const float r = 1.f / sqrtf(wave_sum(q) * (1.f / 512.f) + 1e-5f);
#pragma unroll
    for (int j = 0; j < 8; ++j) v[i][j] *= r;
  }
  asm volatile("s_waitcnt lgkmcnt(0)\n\ts_barrier" ::: "memory");
  {
    const f32x4 s0 = *(const f32x4*)(sc + lane * 8), s1 = *(const f32x4*)(sc + lane * 8 + 4);
    const f32x4 c0 = *(const f32x4*)(sc + 512 + lane * 8), c1 = *(const f32x4*)(sc + 512 + lane * 8 + 4);
#pragma unroll
    for (int i = 0; i < 4; ++i) {
      u32x4 o;
      o[0] = pack2(v[i][0] * c0[0] + s0[0], v[i][1] * c0[1] + s0[1]);
      o[1] = pack2(v[i][2] * c0[2] + s0[2], v[i][3] * c0[3] + s0[3]);
      o[2] = pack2(v[i][4] * c1[0] + s1[0], v[i][5] * c1[1] + s1[1]);
      o[3] = pack2(v[i][6] * c1[2] + s1[2], v[i][7] * c1[3] + s1[3]);
      *(u32x4*)(smem + lane * 528 + (w * 4 + i) * 16) = o;
    }
  }
  asm volatile("s_waitcnt vmcnt(0)" : "+v"(wf[0]), "+v"(wf[1]), "+v"(wf[2]), "+v"(wf[3]));
  if (!iskv)
    asm volatile("" : "+v"(wf[4]), "+v"(wf[5]), "+v"(wf[6]), "+v"(wf[7]), "+v"(wf[8]), "+v"(wf[9]), "+v"(wf[10]), "+v"(wf[11]),
                 "+v"(wf[12]), "+v"(wf[13]), "+v"(wf[14]), "+v"(wf[15]));
  if (iskv && rt < 6) {
    const int i = rt * 512 + tid;
    ((float*)(ws + WS_MODF))[i] = (modp[i] + modp[3072 + i]) + (modp[6144 + i] + modp[9216 + i]);
  }
  __syncthreads();
  TS(1, 2);
  const char* hb = smem + (lane >> 5) * 528 + (lane & 31) * 16;
  char* red = smem + 33792;
  f32x16 acc = zero16();
  if (iskv) {
#pragma unroll
    for (int i = 0; i < 4; ++i) acc = mfma(wf[i], *(const u32x4*)(hb + (w * 4 + i) * 1056), acc);
#pragma unroll
    for (int q = 0; q < 4; ++q)
      *(f32x4*)(red + (w * 4 + q) * 1024 + lane * 16) = f32x4{acc[4 * q], acc[4 * q + 1], acc[4 * q + 2], acc[4 * q + 3]};
    __syncthreads();
    if (tid < 256) {
      const int q = w;
      f32x4 s = {0, 0, 0, 0};
#pragma unroll
      for (int p = 0; p < 8; ++p) s += *(const f32x4*)(red + (p * 4 + q) * 1024 + lane * 16);
      const unsigned p0 = pack2(s[0], s[1]), p1 = pack2(s[2], s[3]);
      if (q < 2) {
        *(u32x2*)(ws + WS_KF + rt * 1024 + lane * 16 + q * 8) = u32x2{p0, p1};
      } else {
        const int t = lane & 31, h = lane >> 5;
        const int d0 = 8 * (q - 2) + 4 * h;
        u16* vp = (u16*)(ws + WS_VF + rt * 1024 + (t >> 4) * 512 + ((t >> 2) & 1) * 256 + d0 * 16) + (((t >> 3) & 1) * 4 + (t & 3));
        vp[0] = (u16)p0; vp[8] = (u16)(p0 >> 16); vp[16] = (u16)p1; vp[24] = (u16)(p1 >> 16);
      }
    }
  } else {
    const int kh = w >> 2, ct = w & 3;
#pragma unroll
    for (int i = 0; i < 16; ++i) acc = mfma(wf[i], *(const u32x4*)(hb + (kh * 16 + i) * 1056), acc);
    f32x4 k0, k1, g0, g1;
    if (kh == 0) {
      k0 = f32x4{acc[0], acc[1], acc[2], acc[3]}; k1 = f32x4{acc[4], acc[5], acc[6], acc[7]};
      g0 = f32x4{acc[8], acc[9], acc[10], acc[11]}; g1 = f32x4{acc[12], acc[13], acc[14], acc[15]};
    } else {
      g0 = f32x4{acc[0], acc[1], acc[2], acc[3]}; g1 = f32x4{acc[4], acc[5], acc[6], acc[7]};
      k0 = f32x4{acc[8], acc[9], acc[10], acc[11]}; k1 = f32x4{acc[12], acc[13], acc[14], acc[15]};
    }
    *(f32x4*)(red + w * 2048 + lane * 16) = g0;
    *(f32x4*)(red + w * 2048 + 1024 + lane * 16) = g1;
    __syncthreads();
    k0 += *(const f32x4*)(red + (w ^ 4) * 2048 + lane * 16);
    k1 += *(const f32x4*)(red + (w ^ 4) * 2048 + 1024 + lane * 16);
    const float sc = 0.25f * 1.4426950408889634f;
    k0 *= sc; k1 *= sc;
    u32x4 o = {pack2(k0[0], k0[1]), pack2(k0[2], k0[3]), pack2(k1[0], k1[1]), pack2(k1[2], k1[3])};
    const int head = (cb * 4 + ct) * 2 + kh;
    st16(ws + WS_QF + ((size_t)(rt * 32 + head) * 64 + lane) * 16, o);
  }
}

__global__ __launch_bounds__(512, 4) void k2_qkv(const u16* __restrict__ x, const u16* __restrict__ wg, const u16* __restrict__ wu,
                                                 char* __restrict__ ws) {
  __shared__ __attribute__((aligned(16))) char smem[33792 + 32768 + 4096];
  const int tid = threadIdx.x, lane = tid & 63;
  const int w = __builtin_amdgcn_readfirstlane(tid >> 6);
  const float* modp = (const float*)(ws + WS_MODP);
  float* sc = (float*)(smem + 33792 + 32768);
  int b = blockIdx.x;
  TS(1, 0);
  if (b < 56) {
    const int up = b >= 28, f0 = (b % 28) * 64;
    const int c8 = tid & 7, kg = tid >> 3;
    const u16* src = (up ? wu : wg) + (size_t)(kg * 8) * 1792 + f0 + c8 * 8;
    u32x4 wr[8];
#pragma unroll
    for (int i = 0; i < 8; ++i) wr[i] = *(const u32x4*)(src + i * 1792);
    {
      const float* mp = modp + 1536 + tid;
      sc[tid] = (mp[0] + mp[3072]) + (mp[6144] + mp[9216]);
      sc[512 + tid] = 1.f + ((mp[512] + mp[3584]) + (mp[6656] + mp[9728]));
    }
    __syncthreads();
    const f32x4 s0 = *(const f32x4*)(sc + kg * 8), s1 = *(const f32x4*)(sc + kg * 8 + 4);
    const f32x4 c0 = *(const f32x4*)(sc + 512 + kg * 8), c1 = *(const f32x4*)(sc + 512 + kg * 8 + 4);
    float au[8], ab[8];
#pragma unroll
    for (int j = 0; j < 8; ++j) au[j] = ab[j] = 0.f;
#pragma unroll
    for (int i = 0; i < 8; ++i) {
      const float c = i < 4 ? c0[i & 3] : c1[i & 3], s = i < 4 ? s0[i & 3] : s1[i & 3];
#pragma unroll
      for (int j = 0; j < 4; ++j) {
        const float lo = bflo(wr[i][j]), hi = bfhi(wr[i][j]);
        au[2 * j] += c * lo; au[2 * j + 1] += c * hi; ab[2 * j] += s * lo; ab[2 * j + 1] += s * hi;
      }
    }
#pragma unroll
    for (int j = 0; j < 8; ++j) { au[j] += dppf<0x128>(au[j]); ab[j] += dppf<0x128>(ab[j]); }
    float* part = (float*)smem;
    if ((lane & 15) < 8) {
      float* p = part + (w * 4 + (lane >> 4)) * 128 + c8 * 16;
      *(f32x4*)p = f32x4{au[0], au[1], au[2], au[3]};
      *(f32x4*)(p + 4) = f32x4{au[4], au[5], au[6], au[7]};
      *(f32x4*)(p + 8) = f32x4{ab[0], ab[1], ab[2], ab[3]};
      *(f32x4*)(p + 12) = f32x4{ab[4], ab[5], ab[6], ab[7]};
    }
    __syncthreads();
    const int col = tid >> 3, seg = tid & 7;
    float u = 0.f, bb = 0.f;
#pragma unroll
    for (int i = 0; i < 4; ++i) {
      const float* p = part + (seg * 4 + i) * 128 + (col >> 3) * 16 + (col & 7);
      u += p[0]; bb += p[8];
    }
    u = red8(u); bb = red8(bb);
    if (seg < 2) {
      unsigned uh, ul, bh, bl;
      split_bf(u, uh, ul); split_bf(bb, bh, bl);
      u32x4 o = {uh | (ul << 16), uh | (ul << 16), bh | (bl << 16), bh | (bl << 16)};
      if (seg) o = u32x4{0, 0, 0, 0};
      const int f = f0 + col, t = f >> 4, m = (f & 15) + 16 * up + 32 * seg;
      *(u32x4*)(ws + WS_WGUF + ((size_t)(t * 33 + 32) * 64 + m) * 16) = o;
    }
    TS(1, 7);
    return;
  }
  b -= 56;
  if (b < 64) qkv_body<true>(x, ws, smem, b, 0);
  else qkv_body<false>(x, ws, smem, b & 63, (b >> 6) - 1);
  TS(1, 6);
}

DEV float xhalf_max(float m) { return fmaxf(m, __shfl_xor(m, 32)); }
DEV float max16(const f32x16& s) {
  float a = fmaxf(fmaxf(s[0], s[1]), s[2]), b = fmaxf(fmaxf(s[3], s[4]), s[5]), c = fmaxf(fmaxf(s[6], s[7]), s[8]);
  float d = fmaxf(fmaxf(s[9], s[10]), s[11]), e = fmaxf(fmaxf(s[12], s[13]), s[14]);
  return fmaxf(fmaxf(fmaxf(a, b), c), fmaxf(fmaxf(d, e), s[15]));
}
#define ATT_LOOP \
  "1:\n\tv_exp_f32 v0, v0\n\tv_exp_f32 v1, v1\n\tv_exp_f32 v2, v2\n\t" \
  "v_exp_f32 v3, v3\n\tv_mfma_f32_32x32x16_bf16 v[48:63], v[88:91], v[96:99], v[48:63]\n\ts_waitcnt lgkmcnt(0)\n\tds_read_b128 v[68:71], v100 offset:2048\n\t" \
  "s_mov_b64 exec, %[msk]\n\tds_read_b128 v[84:87], v101 offset:1024\n\tds_read_b128 v[88:91], v101 offset:1536\n\ts_mov_b64 exec, -1\n\t" \
  "v_cvt_pk_bf16_f32 v92, v0, v1\n\tv_exp_f32 v4, v4\n\tv_exp_f32 v5, v5\n\tv_mfma_f32_32x32x16_bf16 v[16:31], v[72:75], v[64:67], v[32:47]\n\t" \
  "v_cvt_pk_bf16_f32 v93, v2, v3\n\tv_exp_f32 v6, v6\n\tv_exp_f32 v7, v7\n\tv_cvt_pk_bf16_f32 v94, v4, v5\n\t" \
  "v_exp_f32 v8, v8\n\tv_exp_f32 v9, v9\n\tv_cvt_pk_bf16_f32 v95, v6, v7\n\tv_exp_f32 v10, v10\n\t" \
  "v_exp_f32 v11, v11\n\tv_mfma_f32_32x32x16_bf16 v[48:63], v[76:79], v[92:95], v[48:63]\n\tv_cvt_pk_bf16_f32 v96, v8, v9\n\tv_exp_f32 v12, v12\n\t" \
  "v_exp_f32 v13, v13\n\tv_cvt_pk_bf16_f32 v97, v10, v11\n\tv_exp_f32 v14, v14\n\tv_exp_f32 v15, v15\n\t" \
  "v_cvt_pk_bf16_f32 v98, v12, v13\n\tv_cvt_pk_bf16_f32 v99, v14, v15\n\tv_exp_f32 v16, v16\n\tv_exp_f32 v17, v17\n\t" \
  "v_exp_f32 v18, v18\n\tv_exp_f32 v19, v19\n\tv_mfma_f32_32x32x16_bf16 v[48:63], v[80:83], v[96:99], v[48:63]\n\ts_waitcnt lgkmcnt(0)\n\t" \
  "ds_read_b128 v[72:75], v100 offset:3072\n\ts_mov_b64 exec, %[msk]\n\tds_read_b128 v[76:79], v101 offset:2048\n\tds_read_b128 v[80:83], v101 offset:2560\n\t" \
  "s_mov_b64 exec, -1\n\tv_cvt_pk_bf16_f32 v92, v16, v17\n\tv_exp_f32 v20, v20\n\tv_exp_f32 v21, v21\n\t" \
  "v_mfma_f32_32x32x16_bf16 v[0:15], v[68:71], v[64:67], v[32:47]\n\tv_cvt_pk_bf16_f32 v93, v18, v19\n\tv_exp_f32 v22, v22\n\tv_exp_f32 v23, v23\n\t" \
  "v_cvt_pk_bf16_f32 v94, v20, v21\n\tv_exp_f32 v24, v24\n\tv_exp_f32 v25, v25\n\tv_cvt_pk_bf16_f32 v95, v22, v23\n\t" \
  "v_exp_f32 v26, v26\n\tv_exp_f32 v27, v27\n\tv_mfma_f32_32x32x16_bf16 v[48:63], v[84:87], v[92:95], v[48:63]\n\tv_add_u32 v100, 0x800, v100\n\t" \
  "v_add_u32 v101, 0x800, v101\n\ts_sub_u32 %[n], %[n], 1\n\ts_cmp_lg_u32 %[n], 0\n\tv_cvt_pk_bf16_f32 v96, v24, v25\n\t" \
  "v_exp_f32 v28, v28\n\tv_exp_f32 v29, v29\n\tv_cvt_pk_bf16_f32 v97, v26, v27\n\tv_exp_f32 v30, v30\n\t" \
  "v_exp_f32 v31, v31\n\tv_cvt_pk_bf16_f32 v98, v28, v29\n\tv_cvt_pk_bf16_f32 v99, v30, v31\n\ts_cbranch_scc1 1b\n\t" \
  "s_nop 1\n\tv_mfma_f32_32x32x16_bf16 v[48:63], v[88:91], v[96:99], v[48:63]\n\ts_nop 7\n\tv_mov_b32 v96, 0\n\t" \
  "v_mov_b32 v97, 0\n\tv_mov_b32 v98, 0\n\tv_mov_b32 v99, 0\n\ts_waitcnt lgkmcnt(0)\n\t" \
  "s_nop 15\n\t\n\t" \
  ""
#define LDK(dst, addr, off) asm volatile("ds_read_b128 %0, %1 offset:" #off : "=v"(dst) : "v"(addr))
#define LDV(d0, d1, addr, off0, off1)                                                                                   \
  asm volatile("s_mov_b64 exec, %3\n\tds_read_b128 %0, %2 offset:" #off0 "\n\tds_read_b128 %1, %2 offset:" #off1        \
               "\n\ts_mov_b64 exec, -1"                                                                                 \
               : "+v"(d0), "+v"(d1)                                                                                     \
               : "v"(addr), "s"(vmask))
#define LWAIT(a, b, c) asm volatile("s_waitcnt lgkmcnt(0)" : "+v"(a), "+v"(b), "+v"(c))

#define LDH(d0, d1, p) \
  asm volatile("global_load_ushort %0, %2, off\n\tglobal_load_ushort %1, %2, off offset:64" : "=&v"(d0), "=&v"(d1) : "v"(p) : "memory")
__global__ __launch_bounds__(1024, 4) void k3_attn(const u16* __restrict__ wo, const u16* __restrict__ wg, const u16* __restrict__ wu,
                                                   const u16* __restrict__ wd, char* __restrict__ ws) {
  __shared__ __attribute__((aligned(16))) char smem[131072 + 4096];
  const int tid = threadIdx.x, lane = tid & 63;
  const int w = __builtin_amdgcn_readfirstlane(tid >> 6);
  const int rt = blockIdx.x & 63, hg = blockIdx.x >> 6;
  TS(2, 0);
  const int kh = w >> 3;
  const unsigned sbase = (unsigned)(uintptr_t)smem;
  const int head = hg * 8 + (w & 7);
  u32x4 q;
  {
    const unsigned l16 = lane * 16;
    GLD(q, l16, ws + WS_QF + (size_t)(rt * 32 + head) * 1024, 0);
    const char* kp = ws + WS_KF + kh * 32768 + (w & 7) * 1024;
    const unsigned lb = sbase + kh * 32768 + (w & 7) * 1024;
#pragma unroll
    for (int i = 0; i < 4; ++i) {
      dma16(l16, kp + i * 8192, lb + i * 8192);
      dma16(l16, kp + i * 8192 + 65536, lb + i * 8192 + 65536);
    }
  }
  unsigned f0[8], f1[8];
  char* fdst;
  unsigned fstep;
  bool fstore;
  {
    int p = blockIdx.x * 12 + (w < 12 ? w : w - 4);
    fstore = w < 12 && p < 2944;
    if (p >= 2944) p -= 128;
    const int h = lane >> 5, m = lane & 31;
    const u16* src;
    int ld, ka, kb2;
    if (p < 1792) {
      const int t = (p >> 6) * 4 + (p & 1), ks = (p >> 1) & 31;
      ld = 1792; ka = 8 * h; kb2 = ka + 4;
      src = (m < 16 ? wg : wu) + ks * 16 * 1792 + t * 16 + (m & 15);
      fdst = ws + WS_WGUF + (size_t)(t * 33 + ks) * 1024; fstep = 2 * 33 * 1024;
    } else {
      const bool isd = p < 2688;
      const int pp = p - (isd ? 1792 : 2688), t = (pp & 7) * 2, ks = pp >> 3, KS = isd ? 112 : 32;
      ld = 512; ka = 4 * h; kb2 = ka + 8;
      src = (isd ? wd : wo) + ks * 16 * 512 + t * 32 + m;
      fdst = ws + (isd ? WS_WDF : WS_WOF) + (size_t)(t * KS + ks) * 1024; fstep = KS * 1024;
    }
    fdst += lane * 16;
    const u16 *pa = src + ka * ld, *pb = src + kb2 * ld;
    const u16* rp[8] = {pa, pa + ld, pa + 2 * ld, pa + 3 * ld, pb, pb + ld, pb + 2 * ld, pb + 3 * ld};
    asm volatile("s_waitcnt vmcnt(6)\n\ts_barrier" : "+v"(q)::"memory");
#pragma unroll
    for (int j = 0; j < 8; ++j) LDH(f0[j], f1[j], rp[j]);
  }
  const unsigned long long vmask = 0x0000ffff0000ffffull;
  const unsigned kbase = sbase + kh * 32768 + lane * 16;
  const unsigned vbase = sbase + 65536 + kh * 32768 + (lane >> 5) * 256 + (lane & 15) * 16;
  const unsigned one1 = (lane & 27) == 16 ? 0x3f803f80u : 0u;
  const u32x4 ones = {one1, one1, one1, one1};
  u32x4 va0 = ones, va1 = ones, vb0 = ones, vb1 = ones, ka, kb;
  TS(2, 1);
  f32x16 o;
  float off;
  for (int attempt = 0; attempt < 2; ++attempt) {
    float m = -3.0e38f;
    const int ntm = attempt ? 32 : 1;
    for (int t = 0; t < ntm; ++t) {
      const unsigned a = kbase + t * 1024;
      LDK(ka, a, 0);
      LWAIT(ka, va0, va1);
      m = fmaxf(m, max16(mfma(ka, q, zero16())));
    }
    m = xhalf_max(m);
    const float thr = attempt ? __builtin_inff() : 1.f;
    off = m + (attempt ? 0.f : 80.f);
    f32x16 c;
#pragma unroll
    for (int i = 0; i < 16; ++i) c[i] = -off;
    o = zero16();
    unsigned kad = kbase, vad = vbase;
    LDK(ka, kad, 0);
    LDK(kb, kad, 1024);
    LDV(va0, va1, vad, 0, 512);
    asm volatile("s_waitcnt lgkmcnt(0)" : "+v"(ka), "+v"(kb), "+v"(va0), "+v"(va1));
    f32x16 s0 = mfma(ka, q, c);
    u32x4 pz = {0, 0, 0, 0};
    for (int seg = 0; seg < 3; ++seg) {
      if (seg == 1 && attempt == 0) asm volatile("s_waitcnt vmcnt(16)\n\ts_barrier" ::: "memory");
      if (seg == 2 && attempt == 0) {
        asm volatile("s_waitcnt vmcnt(0)"
                     : "+v"(f0[0]), "+v"(f0[1]), "+v"(f0[2]), "+v"(f0[3]), "+v"(f0[4]), "+v"(f0[5]), "+v"(f0[6]), "+v"(f0[7]),
                       "+v"(f1[0]), "+v"(f1[1]), "+v"(f1[2]), "+v"(f1[3]), "+v"(f1[4]), "+v"(f1[5]), "+v"(f1[6]), "+v"(f1[7])::"memory");
        if (fstore) {
          const u32x4 g0 = {f0[0] | (f0[1] << 16), f0[2] | (f0[3] << 16), f0[4] | (f0[5] << 16), f0[6] | (f0[7] << 16)};
          const u32x4 g1 = {f1[0] | (f1[1] << 16), f1[2] | (f1[3] << 16), f1[4] | (f1[5] << 16), f1[6] | (f1[7] << 16)};
          asm volatile("global_store_dwordx4 %0, %1, off sc1\n\tglobal_store_dwordx4 %2, %3, off sc1\n\ts_nop 1"
                       ::"v"(fdst), "v"(g0), "v"(fdst + fstep), "v"(g1) : "memory");
        }
      }
      if (seg) {
        if (__any(o[8] > thr)) {
          const int e = __builtin_amdgcn_frexp_expf(o[8]) + 80;
          const float f = (float)e;
#pragma unroll
          for (int i = 0; i < 9; ++i) o[i] = __builtin_amdgcn_ldexpf(o[i], -e);
          off += f;
#pragma unroll
          for (int i = 0; i < 16; ++i) { c[i] = -off; s0[i] -= f; }
        }
      }
      int np = seg == 0 ? 2 : seg == 1 ? 5 : 9;
      asm volatile("s_nop 15\n\t" ATT_LOOP
                   : "+{v[0:15]}"(s0), "+{v[48:63]}"(o), "+{v[72:75]}"(kb), "+{v[76:79]}"(va0), "+{v[80:83]}"(va1),
                     "+{v[84:87]}"(vb0), "+{v[88:91]}"(vb1), "+{v[96:99]}"(pz), "+{v100}"(kad), "+{v101}"(vad), [n] "+s"(np)
                   : "{v[32:47]}"(c), "{v[64:67]}"(q), [msk] "s"(vmask)
                   : "v16", "v17", "v18", "v19", "v20", "v21", "v22", "v23", "v24", "v25", "v26", "v27", "v28", "v29", "v30", "v31",
                     "v68", "v69", "v70", "v71", "v92", "v93", "v94", "v95", "scc", "memory");
    }
    if (attempt) break;
    float a = 0.f, s = o[8];
#pragma unroll
    for (int i = 0; i < 8; ++i) { a = fmaxf(a, fabsf(o[i])); s += o[i] * 0.f; }
    const bool ok = (s < 3.0e38f) && (a >= 7.8886091e-31f);
    if (!__any(!ok)) break;
  }
  TS(2, 2);
  __syncthreads();
  char* ex = smem + (w & 7) * 3072 + lane * 16;
  if (kh) {
    *(f32x4*)(ex) = f32x4{o[0], o[1], o[2], o[3]};
    *(f32x4*)(ex + 1024) = f32x4{o[4], o[5], o[6], o[7]};
    *(f32x2*)(ex + 2048) = f32x2{o[8], off};
  }
  __syncthreads();
  if (!kh) {
    const f32x4 g0 = *(const f32x4*)(ex), g1 = *(const f32x4*)(ex + 1024);
    const f32x2 g2 = *(const f32x2*)(ex + 2048);
    const float m = fmaxf(off, g2[1]);
    float fk = __builtin_amdgcn_exp2f(off - m), fg = __builtin_amdgcn_exp2f(g2[1] - m);
    const float inv = 1.f / (o[8] * fk + g2[0] * fg);
    fk *= inv; fg *= inv;
    const f32x4 k0 = f32x4{o[0], o[1], o[2], o[3]} * fk + g0 * fg, k1 = f32x4{o[4], o[5], o[6], o[7]} * fk + g1 * fg;
    const u32x4 ov = {pack2(k0[0], k0[1]), pack2(k0[2], k0[3]), pack2(k1[0], k1[1]), pack2(k1[2], k1[3])};
    st16(ws + WS_OF + ((size_t)(rt * 32 + head) * 64 + lane) * 16, ov);
  }
  TS(2, 6);
}

template <int KS, int DEPTH, bool FINAL, int NW>
DEV void k46_gemm(const u16* __restrict__ res, const char* __restrict__ af, const char* __restrict__ wf,
                                                   char* __restrict__ ws, u16* __restrict__ out) {
  __shared__ __attribute__((aligned(16))) float red[NW * 64 * 68];
  const int tid = threadIdx.x, lane = tid & 63;
  const int w = __builtin_amdgcn_readfirstlane(tid >> 6);
  const int b = blockIdx.x;
  const int rb = (b & 7) * 4 + ((b >> 3) & 3), cbk = b >> 5;
  constexpr int N = KS / NW, E = 8 / NW;
  TS(FINAL ? 5 : 3, 0);
  const unsigned l16 = lane * 16;
  const char* ap = af + (size_t)(rb * 2 * KS + w * N) * 1024;
  const char* wp = wf + (size_t)(cbk * 2 * KS + w * N) * 1024;
  u32x4 fa[DEPTH][2], fw[DEPTH][2];
#define LD4(s, k)                                                                                     \
  {                                                                                                   \
    GLD(fa[s][0], l16, ap + ((k) >> 2) * 4096, ((k) & 3) * 1024);                                     \
    GLD(fw[s][0], l16, wp + ((k) >> 2) * 4096, ((k) & 3) * 1024);                                     \
    GLD(fa[s][1], l16, ap + KS * 1024 + ((k) >> 2) * 4096, ((k) & 3) * 1024);                         \
    GLD(fw[s][1], l16, wp + KS * 1024 + ((k) >> 2) * 4096, ((k) & 3) * 1024);                         \
  }
#pragma unroll
  for (int i = 0; i < DEPTH; ++i) LD4(i, i)
  const int cg = tid & 7, r0 = tid >> 3;
  const int col = cbk * 64 + cg * 8;
  const float* modf = (const float*)(ws + WS_MODF);
  u32x4 xr[2], g0u, g1u, c0u, c1u;
  {
    const char* rp = (const char*)(res + (size_t)(rb * 64) * 512 + cbk * 64);
    const unsigned ro = r0 * 1024 + cg * 16;
    GLD(xr[0], ro, rp, 0);
    if (E == 2) { GLD(xr[1], ro, rp + 32768, 0); } else xr[1] = u32x4{0, 0, 0, 0};
    const unsigned mo = cg * 32;
    const char* gp = (const char*)(modf + (FINAL ? 2560 : 1024) + cbk * 64);
    GLD(g0u, mo, gp, 0); GLD(g1u, mo, gp, 16);
    const char* cp = (const char*)(modf + 2048 + cbk * 64);
    GLD(c0u, mo, cp, 0); GLD(c1u, mo, cp, 16);
  }
  f32x16 acc[2][2];
  acc[0][0] = acc[0][1] = acc[1][0] = acc[1][1] = zero16();
#pragma unroll
  for (int k = 0; k < N; ++k) {
    const int s = k % DEPTH;
    const int after = 4 * ((k + DEPTH < N ? k + DEPTH : N) - k - 1) + (k < DEPTH ? 4 + E : 0);
    asm volatile("s_waitcnt vmcnt(%4)" : "+v"(fa[s][0]), "+v"(fa[s][1]), "+v"(fw[s][0]), "+v"(fw[s][1]) : "n"(after));
    if (k == 0) TS(FINAL ? 5 : 3, 1);
    acc[0][0] = mfma(fw[s][0], fa[s][0], acc[0][0]);
    acc[0][1] = mfma(fw[s][0], fa[s][1], acc[0][1]);
    acc[1][0] = mfma(fw[s][1], fa[s][0], acc[1][0]);
    acc[1][1] = mfma(fw[s][1], fa[s][1], acc[1][1]);
    if (k + DEPTH < N) LD4(s, k + DEPTH)
  }
  asm volatile("s_waitcnt vmcnt(0)" : "+v"(xr[0]), "+v"(xr[1]), "+v"(g0u), "+v"(g1u), "+v"(c0u), "+v"(c1u));
  TS(FINAL ? 5 : 3, 2);
  const f32x4 g0 = __builtin_bit_cast(f32x4, g0u), g1 = __builtin_bit_cast(f32x4, g1u);
  const f32x4 c0 = __builtin_bit_cast(f32x4, c0u) + 1.f, c1 = __builtin_bit_cast(f32x4, c1u) + 1.f;
  {
    float* rp = red + w * (64 * 68) + (lane & 31) * 68 + (lane >> 5) * 4;
#pragma unroll
    for (int i = 0; i < 2; ++i)
#pragma unroll
      for (int j = 0; j < 2; ++j)
#pragma unroll
        for (int q = 0; q < 4; ++q)
          *(f32x4*)(rp + j * 32 * 68 + i * 32 + q * 8) =
              f32x4{acc[i][j][4 * q], acc[i][j][4 * q + 1], acc[i][j][4 * q + 2], acc[i][j][4 * q + 3]};
  }
  __syncthreads();
#pragma unroll
  for (int e = 0; e < E; ++e) {
    const int r = r0 + 32 * e, row = rb * 64 + r;
    f32x4 s0 = {0, 0, 0, 0}, s1 = s0;
#pragma unroll
    for (int p = 0; p < NW; ++p) {
      const float* rp = red + p * (64 * 68) + r * 68 + cg * 8;
      s0 += *(const f32x4*)rp; s1 += *(const f32x4*)(rp + 4);
    }
    const f32x4 x0 = {bflo(xr[e][0]), bfhi(xr[e][0]), bflo(xr[e][1]), bfhi(xr[e][1])};
    const f32x4 x1 = {bflo(xr[e][2]), bfhi(xr[e][2]), bflo(xr[e][3]), bfhi(xr[e][3])};
    s0 = x0 + g0 * s0; s1 = x1 + g1 * s1;
    const u32x4 o = {pack2(s0[0], s0[1]), pack2(s0[2], s0[3]), pack2(s1[0], s1[1]), pack2(s1[2], s1[3])};
    if (FINAL) *(u32x4*)(out + (size_t)row * 512 + col) = o; else st16(out + (size_t)row * 512 + col, o);
    if (!FINAL) {
      const f32x4 y0 = s0 * c0, y1 = s1 * c1;
      const u32x4 y = {pack2(y0[0], y0[1]), pack2(y0[2], y0[3]), pack2(y1[0], y1[1]), pack2(y1[2], y1[3])};
      st16(ws + WS_YF + (size_t)((row >> 5) * 32 + (col >> 4)) * 1024 + ((col >> 3) & 1) * 512 + (row & 31) * 16, y);
      const float mean = red8(((s0[0] + s0[1]) + (s0[2] + s0[3])) + ((s1[0] + s1[1]) + (s1[2] + s1[3]))) * (1.f / 64.f);
      s0 -= mean; s1 -= mean;
      s0 *= s0; s1 *= s1;
      const float m2 = red8(((s0[0] + s0[1]) + (s0[2] + s0[3])) + ((s1[0] + s1[1]) + (s1[2] + s1[3])));
      if (cg == 0) *(f32x2*)(ws + WS_STAT + (size_t)(row * 8 + cbk) * 8) = f32x2{mean, m2};
    }
  }
  TS(FINAL ? 5 : 3, 6);
}

extern "C" __global__ __launch_bounds__(K4W * 64, 2) void k4_oproj(const u16* __restrict__ res, char* __restrict__ ws) {
  k46_gemm<32, K4D, false, K4W>(res, ws + WS_OF, ws + WS_WOF, ws, (u16*)(ws + WS_X1));
}

__global__ __launch_bounds__(256, K5B) void k5_ffn(char* __restrict__ ws) {
  __shared__ __attribute__((aligned(16))) char smem[5 * 28672 + 7168];
  const int tid = threadIdx.x, lane = tid & 63;
  const int w = __builtin_amdgcn_readfirstlane(tid >> 6);
  const int b = blockIdx.x, xc = b & 7, idx = b >> 3;
  const int rb = (xc & 1) * 8 + (idx & 7), cbk = (xc >> 1) * 4 + (idx >> 3);
  const int rt = rb * 4 + w, t0 = cbk * 7;
  TS(4, 0);
  const unsigned sbase = (unsigned)(uintptr_t)smem;
  const unsigned l16 = lane * 16;
  const char* wsrc = ws + WS_WGUF + (size_t)t0 * 33 * 1024;
  const char* ysrc = ws + WS_YF + (size_t)rt * 32 * 1024;
  u32x4 st[4], y[32];
  {
    const char* sp = ws + WS_STAT + (size_t)(rt * 32) * 64;
    const unsigned so = (lane & 31) * 64;
    GLD(st[0], so, sp, 0); GLD(st[1], so, sp, 16); GLD(st[2], so, sp, 32); GLD(st[3], so, sp, 48);
    const int i2 = w + 4 < 7 ? w + 4 : 6;
    dma16(l16, wsrc + (w * 33 + 32) * 1024, sbase + 5 * 28672 + w * 1024);
    dma16(l16, wsrc + (i2 * 33 + 32) * 1024, sbase + 5 * 28672 + i2 * 1024);
  }
#define ISSUE(c)                                                                                             \
  {                                                                                                          \
    _Pragma("unroll") for (int i = 0; i < 7; ++i)                                                            \
        dma16(l16, wsrc + (i * 33 + 4 * (c) + w) * 1024, sbase + ((c) % 5) * 28672 + (w * 7 + i) * 1024);    \
    const char* yp = ysrc + (c) * 4096;                                                                      \
    GLD(y[4 * (c)], l16, yp, 0); GLD(y[4 * (c) + 1], l16, yp, 1024);                                         \
    GLD(y[4 * (c) + 2], l16, yp, 2048); GLD(y[4 * (c) + 3], l16, yp, 3072);                                  \
  }
#define WAITB(n, c)                                                                                          \
  asm volatile("s_waitcnt vmcnt(%4)\n\ts_barrier"                                                            \
               : "+v"(y[4 * (c)]), "+v"(y[4 * (c) + 1]), "+v"(y[4 * (c) + 2]), "+v"(y[4 * (c) + 3]) : "n"(n) : "memory")
#define UP 2
  ISSUE(0) ISSUE(1)
  if (UP > 2) ISSUE(2)
  f32x16 acc[7];
#pragma unroll
  for (int i = 0; i < 7; ++i) { acc[i] = zero16(); asm volatile("" : "+v"(acc[i])); }
  const char* lp = smem + lane * 16;
  u32x4 wc[7], wn[7];
  WAITB(11 * (UP - 1), 0);
  asm volatile("" : "+v"(st[0]), "+v"(st[1]), "+v"(st[2]), "+v"(st[3]));
#pragma unroll
  for (int i = 0; i < 7; ++i) wc[i] = *(const u32x4*)(lp + i * 1024);
  u32x4 we[7];
#pragma unroll
  for (int i = 0; i < 7; ++i) we[i] = *(const u32x4*)(lp + 5 * 28672 + i * 1024);
  TS(4, 1);
  float r2;
  u32x4 yx;
  {
    float mb[8], m2 = 0.f, mean = 0.f;
#pragma unroll
    for (int i = 0; i < 4; ++i) {
      mb[2 * i] = __uint_as_float(st[i][0]); mb[2 * i + 1] = __uint_as_float(st[i][2]);
      m2 += __uint_as_float(st[i][1]) + __uint_as_float(st[i][3]);
      mean += mb[2 * i] + mb[2 * i + 1];
    }
    mean *= 0.125f;
    float d = 0.f;
#pragma unroll
    for (int i = 0; i < 8; ++i) d += (mb[i] - mean) * (mb[i] - mean);
    const float var = (m2 + 64.f * d) * (1.f / 512.f) + 1e-5f;
    r2 = 1.f / var;
    unsigned mh, ml, rh, rlo;
    split_bf(-mean, mh, ml); split_bf(sqrtf(var), rh, rlo);
    yx = u32x4{mh | (mh << 16), ml | (ml << 16), rh | (rh << 16), rlo | (rlo << 16)};
    if (lane >= 32) yx = u32x4{0, 0, 0, 0};
  }
#define MFV(c, a, b) asm volatile("v_mfma_f32_32x32x16_bf16 %0, %1, %2, %0" : "+v"(c) : "v"(a), "v"(b))
#define KSTEP(g, CUR, NXT, ic, ib)                                                                                   \
  {                                                                                                          \
    const char* np = lp + ((((g) + 1) / 4) % 5) * 28672 + (((g) + 1) % 4) * 7168;                            \
    _Pragma("unroll") for (int i = 0; i < 7; ++i) {                                                          \
      if ((g) < 31) NXT[i] = *(const u32x4*)(np + i * 1024);                                                          \
      __builtin_amdgcn_sched_barrier(6);                                                                     \
      MFV(acc[i], CUR[i], y[g]);                                                                             \
      __builtin_amdgcn_sched_barrier(6);                                                                     \
      const int n = 7 * (g) + i;                                                                             \
      const bool early = n < (5 - UP) * 11;                                                                  \
      const int lc = early ? UP + n / 11 : (ic), j = early ? n % 11 : (ib) + i;                              \
      if (early || (lc >= 5 && lc < 8)) {                                                                    \
        if (j < 7) dma16(l16, wsrc + (j * 33 + 4 * lc + w) * 1024, sbase + (lc % 5) * 28672 + (w * 7 + j) * 1024); \
        else if (j < 11) GLD(y[(4 * lc + j - 7) & 31], l16, ysrc + lc * 4096, ((j - 7) & 3) * 1024);         \
      }                                                                                                      \
      __builtin_amdgcn_sched_barrier(6);                                                                     \
    }                                                                                                        \
  }
#define CHUNK(c, PRE)                                                                                        \
  KSTEP(4 * (c), wc, wn, (c) + 3, 7) KSTEP(4 * (c) + 1, wn, wc, 0, 0) KSTEP(4 * (c) + 2, wc, wn, 0, 0) PRE KSTEP(4 * (c) + 3, wn, wc, (c) + 4, 0)
#define CORR                                                                                                 \
  { asm volatile("s_nop 3" : "+v"(yx));                                                                                                      \
    _Pragma("unroll") for (int i = 0; i < 7; ++i) MFV(acc[i], we[i], yx);                                     \
    __builtin_amdgcn_sched_barrier(6);                                                                       \
  }
  CHUNK(0, CORR WAITB(11 * UP - 1, 1);)
  CHUNK(1, WAITB(22, 2);)
  TS(4, 3);
  CHUNK(2, WAITB(22, 3);)
  CHUNK(3, WAITB(22, 4);)
  TS(4, 4);
  CHUNK(4, WAITB(22, 5);)
  CHUNK(5, WAITB(11, 6);)
  TS(4, 5);
  CHUNK(6, WAITB(0, 7);)
  CHUNK(7, )
  asm volatile("s_nop 15\n\ts_nop 3" : "+v"(acc[0]), "+v"(acc[1]), "+v"(acc[2]), "+v"(acc[3]), "+v"(acc[4]), "+v"(acc[5]), "+v"(acc[6]));
  TS(4, 2);
  char* hp = ws + WS_HHF + ((size_t)(rt * 112 + t0) * 64 + lane) * 16;
#pragma unroll
  for (int i = 0; i < 7; ++i) {
    const f32x16 a = acc[i];
    float hv[8];
#pragma unroll
    for (int j = 0; j < 8; ++j) {
      float m;
      asm("v_max_f32 %0, 0, %1" : "=v"(m) : "v"(a[j]));
      hv[j] = m * r2 * a[j + 8];
    }
    const u32x4 ho = {pack2(hv[0], hv[1]), pack2(hv[2], hv[3]), pack2(hv[4], hv[5]), pack2(hv[6], hv[7])};
    st16(hp + i * 1024, ho);
  }
  TS(4, 6);
}

extern "C" __global__ __launch_bounds__(K6W * 64, 2) void k6_down(char* __restrict__ ws, u16* __restrict__ out) {
  k46_gemm<112, K6D, true, K6W>((const u16*)(ws + WS_X1), ws + WS_HHF, ws + WS_WDF, ws, out);
}

extern "C" void kernel_launch(void* const* d_in, const int* in_sizes, int n_in, void* d_out, int out_size, void* d_ws,
                              size_t ws_size, hipStream_t stream) {
  const u16* x = (const u16*)d_in[0];
  const u16* wq = (const u16*)d_in[1];
  const u16* wk = (const u16*)d_in[2];
  const u16* wv = (const u16*)d_in[3];
  const u16* wo = (const u16*)d_in[4];
  const u16* wg = (const u16*)d_in[6];
  const u16* wu = (const u16*)d_in[7];
  const u16* wd = (const u16*)d_in[8];
  const u16* cc = (const u16*)d_in[9];
  const u16* wmod = (const u16*)d_in[10];
  char* ws = (char*)d_ws;
    k1_prep<<<296, 256, 0, stream>>>(wq, wk, wv, cc, wmod, ws, x);
  k2_qkv<<<376, 512, 0, stream>>>(x, wg, wu, ws);
  k3_attn<<<256, 1024, 0, stream>>>(wo, wg, wu, wd, ws);
  k4_oproj<<<256, K4W * 64, 0, stream>>>(x, ws);
  k5_ffn<<<256, 256, 0, stream>>>(ws);
  k6_down<<<256, K6W * 64, 0, stream>>>(ws, (u16*)d_out);
}
```

```cpp
#include <hip/hip_runtime.h>
#include <hip/hip_bf16.h>
#include <stdint.h>

#define DEV __device__ __forceinline__
typedef __bf16 bf16x8 __attribute__((ext_vector_type(8)));
typedef __bf16 bf16x2 __attribute__((ext_vector_type(2)));
typedef float f32x16 __attribute__((ext_vector_type(16)));
typedef float f32x4 __attribute__((ext_vector_type(4)));
typedef float f32x2 __attribute__((ext_vector_type(2)));
typedef unsigned u32x4 __attribute__((ext_vector_type(4)));
typedef unsigned u32x2 __attribute__((ext_vector_type(2)));
typedef unsigned short u16;

#define K4W 8
#define K4D 4
#define K6W 4
#define K6D 9
#define K5B 1
#define K6B 2
#define WS_MODP 0x0u
#define WS_MODF 0xC000u
#define WS_STAT 0x10000u
#define WS_WKVF 0x30000u
#define WS_KF 0x40000u
#define WS_VF 0x50000u
#define WS_WQF 0x80000u
#define WS_WOF 0x100000u
#define WS_WGUF 0x200000u
#define WS_WDF 0x600000u
#define WS_QF 0x800000u
#define WS_OF 0xA00000u
#define WS_X1 0xC00000u
#define WS_YF 0xE00000u
#define WS_HHF 0x1000000u

#define TS(k, slot)
DEV unsigned pack2(float a, float b) {
  f32x2 v = {a, b};
  bf16x2 r = __builtin_convertvector(v, bf16x2);
  return __builtin_bit_cast(unsigned, r);
}
DEV float bflo(unsigned u) { return __uint_as_float(u << 16); }
DEV float bfhi(unsigned u) { return __uint_as_float(u & 0xffff0000u); }
DEV f32x16 mfma(u32x4 a, u32x4 b, f32x16 c) {
  return __builtin_amdgcn_mfma_f32_32x32x16_bf16(__builtin_bit_cast(bf16x8, a), __builtin_bit_cast(bf16x8, b), c, 0, 0, 0);
}
template <int CTRL> DEV float dppf(float v) {
  return __builtin_bit_cast(float, __builtin_amdgcn_update_dpp(0, __builtin_bit_cast(int, v), CTRL, 0xf, 0xf, true));
}
DEV float red4(float v) { v += dppf<0xB1>(v); v += dppf<0x4E>(v); return v; }
DEV float red8(float v) { v = red4(v); v += dppf<0x141>(v); return v; }
DEV float red16(float v) { v = red8(v); v += dppf<0x140>(v); return v; }
DEV float rl(float v, int l) { return __builtin_bit_cast(float, __builtin_amdgcn_readlane(__builtin_bit_cast(int, v), l)); }
DEV float wave_sum(float v) { v = red16(v); return (rl(v, 0) + rl(v, 16)) + (rl(v, 32) + rl(v, 48)); }
#define GLD(dst, voff, sptr, imm) \
  asm volatile("global_load_dwordx4 %0, %1, %2 offset:%3" : "=v"(dst) : "v"(voff), "s"(sptr), "n"(imm) : "memory")
#define GLD1(dst, voff, sptr, imm) \
  asm volatile("global_load_dword %0, %1, %2 offset:%3" : "=v"(dst) : "v"(voff), "s"(sptr), "n"(imm) : "memory")
DEV void dma16(unsigned voff, const char* sptr, unsigned ldsoff) {
  asm volatile("s_mov_b32 m0, %2\n\ts_nop 0\n\tglobal_load_lds_dwordx4 %0, %1" ::"v"(voff), "s"(sptr), "s"(ldsoff) : "memory");
}
DEV void st16(void* p, u32x4 v) { asm volatile("global_store_dwordx4 %0, %1, off sc1\n\ts_nop 2" ::"v"(p), "v"(v) : "memory"); }
DEV f32x16 zero16() { f32x16 z; for (int i = 0; i < 16; ++i) z[i] = 0.f; return z; }

__global__ __launch_bounds__(256) void k1_prep(const u16* __restrict__ wq, const u16* __restrict__ wk, const u16* __restrict__ wv,
                                               const u16* __restrict__ cc, const u16* __restrict__ wmod, char* __restrict__ ws) {
  __shared__ __attribute__((aligned(16))) u16 tile[64 * 136];
  const int tid = threadIdx.x, lane = tid & 63, w = tid >> 6;
  int b = blockIdx.x;
  TS(0, 0);
  if (b < 192) {
    const int cbk = b % 48, kq = b / 48;
    const int c8 = tid & 7, kg = tid >> 3;
    const int k0 = kq * 128 + kg * 4;
    u32x4 wr[4];
#pragma unroll
    for (int i = 0; i < 4; ++i) wr[i] = *(const u32x4*)(wmod + (size_t)(k0 + i) * 3072 + cbk * 64 + c8 * 8);
    u32x2 cr = *(const u32x2*)(cc + k0);
    float cv[4] = {bflo(cr[0]), bfhi(cr[0]), bflo(cr[1]), bfhi(cr[1])};
    float acc[8];
#pragma unroll
    for (int j = 0; j < 8; ++j) acc[j] = 0.f;
#pragma unroll
    for (int i = 0; i < 4; ++i) {
      float s = cv[i] / (1.f + __expf(-cv[i]));
#pragma unroll
      for (int j = 0; j < 4; ++j) { acc[2 * j] += s * bflo(wr[i][j]); acc[2 * j + 1] += s * bfhi(wr[i][j]); }
    }
    float* part = (float*)tile;
#pragma unroll
    for (int j = 0; j < 8; ++j) acc[j] += dppf<0x128>(acc[j]);
    if ((lane & 15) < 8) {
      float* p = part + (w * 4 + (lane >> 4)) * 64 + c8 * 8;
      *(f32x4*)p = f32x4{acc[0], acc[1], acc[2], acc[3]};
      *(f32x4*)(p + 4) = f32x4{acc[4], acc[5], acc[6], acc[7]};
    }
    __syncthreads();
    {
      const int col = tid >> 2, seg = tid & 3;
      float s = 0.f;
#pragma unroll
      for (int i = 0; i < 4; ++i) s += part[(seg * 4 + i) * 64 + col];
      s = red4(s);
      if (seg == 0) ((float*)(ws + WS_MODP))[kq * 3072 + cbk * 64 + col] = s;
    }
    TS(0, 7);
    return;
  }
  b -= 192;
  if (b < 8) {
    const int ks = b * 4 + w, h = lane >> 5, m = lane & 31;
    const u16* src = (m < 16 ? wk : wv) + (m & 15) + (ks * 16 + h * 8) * 16;
    u32x4 o;
#pragma unroll
    for (int j = 0; j < 4; ++j) o[j] = (unsigned)src[(2 * j) * 16] | ((unsigned)src[(2 * j + 1) * 16] << 16);
    *(u32x4*)(ws + WS_WKVF + (ks * 64 + lane) * 16) = o;
    return;
  }
  b -= 8;
  const int kb = b >> 2, nb = b & 3;
  const u16* sl = wq + kb * 64 * 512 + nb * 128;
  u32x4 r[4];
#pragma unroll
  for (int i = 0; i < 4; ++i) {
    const int p = tid + 256 * i, row = p >> 4, c = p & 15;
    r[i] = *(const u32x4*)(sl + row * 512 + c * 8);
  }
#pragma unroll
  for (int i = 0; i < 4; ++i) {
    const int p = tid + 256 * i, row = p >> 4, c = p & 15;
    *(u32x4*)(tile + row * 136 + c * 8) = r[i];
  }
  __syncthreads();
  const int h = lane >> 5, m = lane & 31;
  const int ka = w * 16 + 8 * h, kb2 = ka + 4;
#pragma unroll
  for (int tt = 0; tt < 4; ++tt) {
    const int n = tt * 32 + m;
    const u16* pa = tile + ka * 136 + n;
    const u16* pb = tile + kb2 * 136 + n;
    u32x4 o;
    o[0] = (unsigned)pa[0] | ((unsigned)pa[136] << 16);
    o[1] = (unsigned)pa[272] | ((unsigned)pa[408] << 16);
    o[2] = (unsigned)pb[0] | ((unsigned)pb[136] << 16);
    o[3] = (unsigned)pb[272] | ((unsigned)pb[408] << 16);
    *(u32x4*)(ws + WS_WQF + ((size_t)((nb * 4 + tt) * 32 + kb * 4 + w) * 64 + lane) * 16) = o;
  }
  TS(0, 6);
}

DEV void split_bf(float v, unsigned& hi, unsigned& lo) {
  unsigned p = pack2(v, 0.f) & 0xffffu;
  float r = v - __uint_as_float(p << 16);
  hi = p; lo = pack2(r, 0.f) & 0xffffu;
}

template <bool iskv> DEV void qkv_body(const u16* __restrict__ x, char* __restrict__ ws, char* smem, const int rt, const int cb) {
  const int tid = threadIdx.x, lane = tid & 63;
  const int w = __builtin_amdgcn_readfirstlane(tid >> 6);
  const float* modp = (const float*)(ws + WS_MODP);
  float* sc = (float*)(smem + 33792 + 32768);
  const unsigned l16 = lane * 16;
  u32x4 xr[4];
  {
    const char* xp = (const char*)(x + (size_t)(rt * 32 + w * 4) * 512);
    GLD(xr[0], l16, xp, 0); GLD(xr[1], l16, xp, 1024); GLD(xr[2], l16, xp, 2048); GLD(xr[3], l16, xp, 3072);
  }
  float mp[8];
#pragma unroll
  for (int i = 0; i < 4; ++i) { GLD1(mp[i], tid * 4, modp + i * 3072, 0); GLD1(mp[4 + i], tid * 4, modp + i * 3072, 2048); }
  u32x4 wf[16];
  {
    const char* p = iskv ? ws + WS_WKVF + w * 4096 : ws + WS_WQF + (size_t)((cb * 4 + (w & 3)) * 32 + (w >> 2) * 16) * 1024;
    GLD(wf[0], l16, p, 0); GLD(wf[1], l16, p, 1024); GLD(wf[2], l16, p, 2048); GLD(wf[3], l16, p, 3072);
    if (!iskv) {
#pragma unroll
      for (int i = 1; i < 4; ++i) {
        const char* q = p + i * 4096;
        GLD(wf[4 * i], l16, q, 0); GLD(wf[4 * i + 1], l16, q, 1024); GLD(wf[4 * i + 2], l16, q, 2048); GLD(wf[4 * i + 3], l16, q, 3072);
      }
      asm volatile("s_waitcnt vmcnt(16)" : "+v"(xr[0]), "+v"(xr[1]), "+v"(xr[2]), "+v"(xr[3]), "+v"(mp[0]), "+v"(mp[1]), "+v"(mp[2]),
                   "+v"(mp[3]), "+v"(mp[4]), "+v"(mp[5]), "+v"(mp[6]), "+v"(mp[7]));
    } else {
      asm volatile("s_waitcnt vmcnt(4)" : "+v"(xr[0]), "+v"(xr[1]), "+v"(xr[2]), "+v"(xr[3]), "+v"(mp[0]), "+v"(mp[1]), "+v"(mp[2]),
                   "+v"(mp[3]), "+v"(mp[4]), "+v"(mp[5]), "+v"(mp[6]), "+v"(mp[7]));
    }
  }
  TS(1, 1);
  sc[tid] = (mp[0] + mp[1]) + (mp[2] + mp[3]);
  sc[512 + tid] = 1.f + ((mp[4] + mp[5]) + (mp[6] + mp[7]));
  float v[4][8];
#pragma unroll
  for (int i = 0; i < 4; ++i) {
#pragma unroll
    for (int j = 0; j < 4; ++j) { v[i][2 * j] = bflo(xr[i][j]); v[i][2 * j + 1] = bfhi(xr[i][j]); }
    float s = ((v[i][0] + v[i][1]) + (v[i][2] + v[i][3])) + ((v[i][4] + v[i][5]) + (v[i][6] + v[i][7]));
    const float mean = wave_sum(s) * (1.f / 512.f);
    float q = 0.f;
#pragma unroll
    for (int j = 0; j < 8; ++j) { v[i][j] -= mean; q += v[i][j] * v[i][j]; }
    const float r = 1.f / sqrtf(wave_sum(q) * (1.f / 512.f) + 1e-5f);
#pragma unroll
    for (int j = 0; j < 8; ++j) v[i][j] *= r;
  }
  asm volatile("s_waitcnt lgkmcnt(0)\n\ts_barrier" ::: "memory");
  {
    const f32x4 s0 = *(const f32x4*)(sc + lane * 8), s1 = *(const f32x4*)(sc + lane * 8 + 4);
    const f32x4 c0 = *(const f32x4*)(sc + 512 + lane * 8), c1 = *(const f32x4*)(sc + 512 + lane * 8 + 4);
#pragma unroll
    for (int i = 0; i < 4; ++i) {
      u32x4 o;
      o[0] = pack2(v[i][0] * c0[0] + s0[0], v[i][1] * c0[1] + s0[1]);
      o[1] = pack2(v[i][2] * c0[2] + s0[2], v[i][3] * c0[3] + s0[3]);
      o[2] = pack2(v[i][4] * c1[0] + s1[0], v[i][5] * c1[1] + s1[1]);
      o[3] = pack2(v[i][6] * c1[2] + s1[2], v[i][7] * c1[3] + s1[3]);
      *(u32x4*)(smem + lane * 528 + (w * 4 + i) * 16) = o;
    }
  }
  asm volatile("s_waitcnt vmcnt(0)" : "+v"(wf[0]), "+v"(wf[1]), "+v"(wf[2]), "+v"(wf[3]));
  if (!iskv)
    asm volatile("" : "+v"(wf[4]), "+v"(wf[5]), "+v"(wf[6]), "+v"(wf[7]), "+v"(wf[8]), "+v"(wf[9]), "+v"(wf[10]), "+v"(wf[11]),
                 "+v"(wf[12]), "+v"(wf[13]), "+v"(wf[14]), "+v"(wf[15]));
  if (iskv && rt < 6) {
    const int i = rt * 512 + tid;
    ((float*)(ws + WS_MODF))[i] = (modp[i] + modp[3072 + i]) + (modp[6144 + i] + modp[9216 + i]);
  }
  __syncthreads();
  TS(1, 2);
  const char* hb = smem + (lane >> 5) * 528 + (lane & 31) * 16;
  char* red = smem + 33792;
  f32x16 acc = zero16();
  if (iskv) {
#pragma unroll
    for (int i = 0; i < 4; ++i) acc = mfma(wf[i], *(const u32x4*)(hb + (w * 4 + i) * 1056), acc);
#pragma unroll
    for (int q = 0; q < 4; ++q)
      *(f32x4*)(red + (w * 4 + q) * 1024 + lane * 16) = f32x4{acc[4 * q], acc[4 * q + 1], acc[4 * q + 2], acc[4 * q + 3]};
    __syncthreads();
    if (tid < 256) {
      const int q = w;
      f32x4 s = {0, 0, 0, 0};
#pragma unroll
      for (int p = 0; p < 8; ++p) s += *(const f32x4*)(red + (p * 4 + q) * 1024 + lane * 16);
      const unsigned p0 = pack2(s[0], s[1]), p1 = pack2(s[2], s[3]);
      if (q < 2) {
        *(u32x2*)(ws + WS_KF + rt * 1024 + lane * 16 + q * 8) = u32x2{p0, p1};
      } else {
        const int t = lane & 31, h = lane >> 5;
        const int d0 = 8 * (q - 2) + 4 * h;
        u16* vp = (u16*)(ws + WS_VF + rt * 1024 + (t >> 4) * 512 + ((t >> 2) & 1) * 256 + d0 * 16) + (((t >> 3) & 1) * 4 + (t & 3));
        vp[0] = (u16)p0; vp[8] = (u16)(p0 >> 16); vp[16] = (u16)p1; vp[24] = (u16)(p1 >> 16);
      }
    }
  } else {
    const int kh = w >> 2, ct = w & 3;
#pragma unroll
    for (int i = 0; i < 16; ++i) acc = mfma(wf[i], *(const u32x4*)(hb + (kh * 16 + i) * 1056), acc);
    f32x4 k0, k1, g0, g1;
    if (kh == 0) {
      k0 = f32x4{acc[0], acc[1], acc[2], acc[3]}; k1 = f32x4{acc[4], acc[5], acc[6], acc[7]};
      g0 = f32x4{acc[8], acc[9], acc[10], acc[11]}; g1 = f32x4{acc[12], acc[13], acc[14], acc[15]};
    } else {
      g0 = f32x4{acc[0], acc[1], acc[2], acc[3]}; g1 = f32x4{acc[4], acc[5], acc[6], acc[7]};
      k0 = f32x4{acc[8], acc[9], acc[10], acc[11]}; k1 = f32x4{acc[12], acc[13], acc[14], acc[15]};
    }
    *(f32x4*)(red + w * 2048 + lane * 16) = g0;
    *(f32x4*)(red + w * 2048 + 1024 + lane * 16) = g1;
    __syncthreads();
    k0 += *(const f32x4*)(red + (w ^ 4) * 2048 + lane * 16);
    k1 += *(const f32x4*)(red + (w ^ 4) * 2048 + 1024 + lane * 16);
    const float sc = 0.25f * 1.4426950408889634f;
    k0 *= sc; k1 *= sc;
    u32x4 o = {pack2(k0[0], k0[1]), pack2(k0[2], k0[3]), pack2(k1[0], k1[1]), pack2(k1[2], k1[3])};
    const int head = (cb * 4 + ct) * 2 + kh;
    st16(ws + WS_QF + ((size_t)(rt * 32 + head) * 64 + lane) * 16, o);
  }
}

__global__ __launch_bounds__(512, 4) void k2_qkv(const u16* __restrict__ x, const u16* __restrict__ wg, const u16* __restrict__ wu,
                                                 char* __restrict__ ws) {
  __shared__ __attribute__((aligned(16))) char smem[33792 + 32768 + 4096];
  const int tid = threadIdx.x, lane = tid & 63;
  const int w = __builtin_amdgcn_readfirstlane(tid >> 6);
  const float* modp = (const float*)(ws + WS_MODP);
  float* sc = (float*)(smem + 33792 + 32768);
  int b = blockIdx.x;
  TS(1, 0);
  if (b < 56) {
    const int up = b >= 28, f0 = (b % 28) * 64;
    const int c8 = tid & 7, kg = tid >> 3;
    const u16* src = (up ? wu : wg) + (size_t)(kg * 8) * 1792 + f0 + c8 * 8;
    u32x4 wr[8];
#pragma unroll
    for (int i = 0; i < 8; ++i) wr[i] = *(const u32x4*)(src + i * 1792);
    {
      const float* mp = modp + 1536 + tid;
      sc[tid] = (mp[0] + mp[3072]) + (mp[6144] + mp[9216]);
      sc[512 + tid] = 1.f + ((mp[512] + mp[3584]) + (mp[6656] + mp[9728]));
    }
    __syncthreads();
    const f32x4 s0 = *(const f32x4*)(sc + kg * 8), s1 = *(const f32x4*)(sc + kg * 8 + 4);
    const f32x4 c0 = *(const f32x4*)(sc + 512 + kg * 8), c1 = *(const f32x4*)(sc + 512 + kg * 8 + 4);
    float au[8], ab[8];
#pragma unroll
    for (int j = 0; j < 8; ++j) au[j] = ab[j] = 0.f;
#pragma unroll
    for (int i = 0; i < 8; ++i) {
      const float c = i < 4 ? c0[i & 3] : c1[i & 3], s = i < 4 ? s0[i & 3] : s1[i & 3];
#pragma unroll
      for (int j = 0; j < 4; ++j) {
        const float lo = bflo(wr[i][j]), hi = bfhi(wr[i][j]);
        au[2 * j] += c * lo; au[2 * j + 1] += c * hi; ab[2 * j] += s * lo; ab[2 * j + 1] += s * hi;
      }
    }
#pragma unroll
    for (int j = 0; j < 8; ++j) { au[j] += dppf<0x128>(au[j]); ab[j] += dppf<0x128>(ab[j]); }
    float* part = (float*)smem;
    if ((lane & 15) < 8) {
      float* p = part + (w * 4 + (lane >> 4)) * 128 + c8 * 16;
      *(f32x4*)p = f32x4{au[0], au[1], au[2], au[3]};
      *(f32x4*)(p + 4) = f32x4{au[4], au[5], au[6], au[7]};
      *(f32x4*)(p + 8) = f32x4{ab[0], ab[1], ab[2], ab[3]};
      *(f32x4*)(p + 12) = f32x4{ab[4], ab[5], ab[6], ab[7]};
    }
    __syncthreads();
    const int col = tid >> 3, seg = tid & 7;
    float u = 0.f, bb = 0.f;
#pragma unroll
    for (int i = 0; i < 4; ++i) {
      const float* p = part + (seg * 4 + i) * 128 + (col >> 3) * 16 + (col & 7);
      u += p[0]; bb += p[8];
    }
    u = red8(u); bb = red8(bb);
    if (seg < 2) {
      unsigned uh, ul, bh, bl;
      split_bf(u, uh, ul); split_bf(bb, bh, bl);
      u32x4 o = {uh | (ul << 16), uh | (ul << 16), bh | (bl << 16), bh | (bl << 16)};
      if (seg) o = u32x4{0, 0, 0, 0};
      const int f = f0 + col, t = f >> 4, m = (f & 15) + 16 * up + 32 * seg;
      *(u32x4*)(ws + WS_WGUF + ((size_t)(t * 33 + 32) * 64 + m) * 16) = o;
    }
    TS(1, 7);
    return;
  }
  b -= 56;
  if (b < 64) qkv_body<true>(x, ws, smem, b, 0);
  else qkv_body<false>(x, ws, smem, b & 63, (b >> 6) - 1);
  TS(1, 6);
}

DEV float xhalf_max(float m) { return fmaxf(m, __shfl_xor(m, 32)); }
DEV float max16(const f32x16& s) {
  float a = fmaxf(fmaxf(s[0], s[1]), s[2]), b = fmaxf(fmaxf(s[3], s[4]), s[5]), c = fmaxf(fmaxf(s[6], s[7]), s[8]);
  float d = fmaxf(fmaxf(s[9], s[10]), s[11]), e = fmaxf(fmaxf(s[12], s[13]), s[14]);
  return fmaxf(fmaxf(fmaxf(a, b), c), fmaxf(fmaxf(d, e), s[15]));
}
#define ATT_LOOP \
  "1:\n\tv_exp_f32 v0, v0\n\tv_exp_f32 v1, v1\n\tv_exp_f32 v2, v2\n\t" \
  "v_exp_f32 v3, v3\n\tv_mfma_f32_32x32x16_bf16 v[48:63], v[88:91], v[96:99], v[48:63]\n\ts_waitcnt lgkmcnt(0)\n\tds_read_b128 v[68:71], v100 offset:2048\n\t" \
  "s_mov_b64 exec, %[msk]\n\tds_read_b128 v[84:87], v101 offset:1024\n\tds_read_b128 v[88:91], v101 offset:1536\n\ts_mov_b64 exec, -1\n\t" \
  "v_cvt_pk_bf16_f32 v92, v0, v1\n\tv_exp_f32 v4, v4\n\tv_exp_f32 v5, v5\n\tv_mfma_f32_32x32x16_bf16 v[16:31], v[72:75], v[64:67], v[32:47]\n\t" \
  "v_cvt_pk_bf16_f32 v93, v2, v3\n\tv_exp_f32 v6, v6\n\tv_exp_f32 v7, v7\n\tv_cvt_pk_bf16_f32 v94, v4, v5\n\t" \
  "v_exp_f32 v8, v8\n\tv_exp_f32 v9, v9\n\tv_cvt_pk_bf16_f32 v95, v6, v7\n\tv_exp_f32 v10, v10\n\t" \
  "v_exp_f32 v11, v11\n\tv_mfma_f32_32x32x16_bf16 v[48:63], v[76:79], v[92:95], v[48:63]\n\tv_cvt_pk_bf16_f32 v96, v8, v9\n\tv_exp_f32 v12, v12\n\t" \
  "v_exp_f32 v13, v13\n\tv_cvt_pk_bf16_f32 v97, v10, v11\n\tv_exp_f32 v14, v14\n\tv_exp_f32 v15, v15\n\t" \
  "v_cvt_pk_bf16_f32 v98, v12, v13\n\tv_cvt_pk_bf16_f32 v99, v14, v15\n\tv_exp_f32 v16, v16\n\tv_exp_f32 v17, v17\n\t" \
  "v_exp_f32 v18, v18\n\tv_exp_f32 v19, v19\n\tv_mfma_f32_32x32x16_bf16 v[48:63], v[80:83], v[96:99], v[48:63]\n\ts_waitcnt lgkmcnt(0)\n\t" \
  "ds_read_b128 v[72:75], v100 offset:3072\n\ts_mov_b64 exec, %[msk]\n\tds_read_b128 v[76:79], v101 offset:2048\n\tds_read_b128 v[80:83], v101 offset:2560\n\t" \
  "s_mov_b64 exec, -1\n\tv_cvt_pk_bf16_f32 v92, v16, v17\n\tv_exp_f32 v20, v20\n\tv_exp_f32 v21, v21\n\t" \
  "v_mfma_f32_32x32x16_bf16 v[0:15], v[68:71], v[64:67], v[32:47]\n\tv_cvt_pk_bf16_f32 v93, v18, v19\n\tv_exp_f32 v22, v22\n\tv_exp_f32 v23, v23\n\t" \
  "v_cvt_pk_bf16_f32 v94, v20, v21\n\tv_exp_f32 v24, v24\n\tv_exp_f32 v25, v25\n\tv_cvt_pk_bf16_f32 v95, v22, v23\n\t" \
  "v_exp_f32 v26, v26\n\tv_exp_f32 v27, v27\n\tv_mfma_f32_32x32x16_bf16 v[48:63], v[84:87], v[92:95], v[48:63]\n\tv_add_u32 v100, 0x800, v100\n\t" \
  "v_add_u32 v101, 0x800, v101\n\ts_sub_u32 %[n], %[n], 1\n\ts_cmp_lg_u32 %[n], 0\n\tv_cvt_pk_bf16_f32 v96, v24, v25\n\t" \
  "v_exp_f32 v28, v28\n\tv_exp_f32 v29, v29\n\tv_cvt_pk_bf16_f32 v97, v26, v27\n\tv_exp_f32 v30, v30\n\t" \
  "v_exp_f32 v31, v31\n\tv_cvt_pk_bf16_f32 v98, v28, v29\n\tv_cvt_pk_bf16_f32 v99, v30, v31\n\ts_cbranch_scc1 1b\n\t" \
  "s_nop 1\n\tv_mfma_f32_32x32x16_bf16 v[48:63], v[88:91], v[96:99], v[48:63]\n\ts_nop 7\n\tv_mov_b32 v96, 0\n\t" \
  "v_mov_b32 v97, 0\n\tv_mov_b32 v98, 0\n\tv_mov_b32 v99, 0\n\ts_waitcnt lgkmcnt(0)\n\t" \
  "s_nop 15\n\t\n\t" \
  ""
#define LDK(dst, addr, off) asm volatile("ds_read_b128 %0, %1 offset:" #off : "=v"(dst) : "v"(addr))
#define LDV(d0, d1, addr, off0, off1)                                                                                   \
  asm volatile("s_mov_b64 exec, %3\n\tds_read_b128 %0, %2 offset:" #off0 "\n\tds_read_b128 %1, %2 offset:" #off1        \
               "\n\ts_mov_b64 exec, -1"                                                                                 \
               : "+v"(d0), "+v"(d1)                                                                                     \
               : "v"(addr), "s"(vmask))
#define LWAIT(a, b, c) asm volatile("s_waitcnt lgkmcnt(0)" : "+v"(a), "+v"(b), "+v"(c))

#define LDH(d0, d1, p) \
  asm volatile("global_load_ushort %0, %2, off\n\tglobal_load_ushort %1, %2, off offset:64" : "=&v"(d0), "=&v"(d1) : "v"(p) : "memory")
__global__ __launch_bounds__(1024, 4) void k3_attn(const u16* __restrict__ wo, const u16* __restrict__ wg, const u16* __restrict__ wu,
                                                   const u16* __restrict__ wd, char* __restrict__ ws) {
  __shared__ __attribute__((aligned(16))) char smem[131072 + 4096];
  const int tid = threadIdx.x, lane = tid & 63;
  const int w = __builtin_amdgcn_readfirstlane(tid >> 6);
  const int rt = blockIdx.x & 63, hg = blockIdx.x >> 6;
  TS(2, 0);
  const int kh = w >> 3;
  const unsigned sbase = (unsigned)(uintptr_t)smem;
  const int head = hg * 8 + (w & 7);
  u32x4 q;
  {
    const unsigned l16 = lane * 16;
    GLD(q, l16, ws + WS_QF + (size_t)(rt * 32 + head) * 1024, 0);
    const char* kp = ws + WS_KF + kh * 32768 + (w & 7) * 1024;
    const unsigned lb = sbase + kh * 32768 + (w & 7) * 1024;
#pragma unroll
    for (int i = 0; i < 4; ++i) {
      dma16(l16, kp + i * 8192, lb + i * 8192);
      dma16(l16, kp + i * 8192 + 65536, lb + i * 8192 + 65536);
    }
  }
  unsigned f0[8], f1[8];
  char* fdst;
  unsigned fstep;
  bool fstore;
  {
    int p = blockIdx.x * 12 + (w < 12 ? w : w - 4);
    fstore = w < 12 && p < 2944;
    if (p >= 2944) p -= 128;
    const int h = lane >> 5, m = lane & 31;
    const u16* src;
    int ld, ka, kb2;
    if (p < 1792) {
      const int t = (p >> 6) * 4 + (p & 1), ks = (p >> 1) & 31;
      ld = 1792; ka = 8 * h; kb2 = ka + 4;
      src = (m < 16 ? wg : wu) + ks * 16 * 1792 + t * 16 + (m & 15);
      fdst = ws + WS_WGUF + (size_t)(t * 33 + ks) * 1024; fstep = 2 * 33 * 1024;
    } else {
      const bool isd = p < 2688;
      const int pp = p - (isd ? 1792 : 2688), t = (pp & 7) * 2, ks = pp >> 3, KS = isd ? 112 : 32;
      ld = 512; ka = 4 * h; kb2 = ka + 8;
      src = (isd ? wd : wo) + ks * 16 * 512 + t * 32 + m;
      fdst = ws + (isd ? WS_WDF : WS_WOF) + (size_t)(t * KS + ks) * 1024; fstep = KS * 1024;
    }
    fdst += lane * 16;
    const u16 *pa = src + ka * ld, *pb = src + kb2 * ld;
    const u16* rp[8] = {pa, pa + ld, pa + 2 * ld, pa + 3 * ld, pb, pb + ld, pb + 2 * ld, pb + 3 * ld};
    asm volatile("s_waitcnt vmcnt(6)\n\ts_barrier" : "+v"(q)::"memory");
#pragma unroll
    for (int j = 0; j < 8; ++j) LDH(f0[j], f1[j], rp[j]);
  }
  const unsigned long long vmask = 0x0000ffff0000ffffull;
  const unsigned kbase = sbase + kh * 32768 + lane * 16;
  const unsigned vbase = sbase + 65536 + kh * 32768 + (lane >> 5) * 256 + (lane & 15) * 16;
  const unsigned one1 = (lane & 27) == 16 ? 0x3f803f80u : 0u;
  const u32x4 ones = {one1, one1, one1, one1};
  u32x4 va0 = ones, va1 = ones, vb0 = ones, vb1 = ones, ka, kb;
  TS(2, 1);
  f32x16 o;
  float off;
  for (int attempt = 0; attempt < 2; ++attempt) {
    float m = -3.0e38f;
    const int ntm = attempt ? 32 : 1;
    for (int t = 0; t < ntm; ++t) {
      const unsigned a = kbase + t * 1024;
      LDK(ka, a, 0);
      LWAIT(ka, va0, va1);
      m = fmaxf(m, max16(mfma(ka, q, zero16())));
    }
    m = xhalf_max(m);
    const float thr = attempt ? __builtin_inff() : 1.f;
    off = m + (attempt ? 0.f : 80.f);
    f32x16 c;
#pragma unroll
    for (int i = 0; i < 16; ++i) c[i] = -off;
    o = zero16();
    unsigned kad = kbase, vad = vbase;
    LDK(ka, kad, 0);
    LDK(kb, kad, 1024);
    LDV(va0, va1, vad, 0, 512);
    asm volatile("s_waitcnt lgkmcnt(0)" : "+v"(ka), "+v"(kb), "+v"(va0), "+v"(va1));
    f32x16 s0 = mfma(ka, q, c);
    u32x4 pz = {0, 0, 0, 0};
    for (int seg = 0; seg < 3; ++seg) {
      if (seg == 1 && attempt == 0) asm volatile("s_waitcnt vmcnt(16)\n\ts_barrier" ::: "memory");
      if (seg == 2 && attempt == 0) {
        asm volatile("s_waitcnt vmcnt(0)"
                     : "+v"(f0[0]), "+v"(f0[1]), "+v"(f0[2]), "+v"(f0[3]), "+v"(f0[4]), "+v"(f0[5]), "+v"(f0[6]), "+v"(f0[7]),
                       "+v"(f1[0]), "+v"(f1[1]), "+v"(f1[2]), "+v"(f1[3]), "+v"(f1[4]), "+v"(f1[5]), "+v"(f1[6]), "+v"(f1[7])::"memory");
        if (fstore) {
          const u32x4 g0 = {f0[0] | (f0[1] << 16), f0[2] | (f0[3] << 16), f0[4] | (f0[5] << 16), f0[6] | (f0[7] << 16)};
          const u32x4 g1 = {f1[0] | (f1[1] << 16), f1[2] | (f1[3] << 16), f1[4] | (f1[5] << 16), f1[6] | (f1[7] << 16)};
          asm volatile("global_store_dwordx4 %0, %1, off sc1\n\tglobal_store_dwordx4 %2, %3, off sc1\n\ts_nop 1"
                       ::"v"(fdst), "v"(g0), "v"(fdst + fstep), "v"(g1) : "memory");
        }
      }
      if (seg) {
        if (__any(o[8] > thr)) {
          const int e = __builtin_amdgcn_frexp_expf(o[8]) + 80;
          const float f = (float)e;
#pragma unroll
          for (int i = 0; i < 9; ++i) o[i] = __builtin_amdgcn_ldexpf(o[i], -e);
          off += f;
#pragma unroll
          for (int i = 0; i < 16; ++i) { c[i] = -off; s0[i] -= f; }
        }
      }
      int np = seg == 0 ? 2 : seg == 1 ? 5 : 9;
      asm volatile("s_nop 15\n\t" ATT_LOOP
                   : "+{v[0:15]}"(s0), "+{v[48:63]}"(o), "+{v[72:75]}"(kb), "+{v[76:79]}"(va0), "+{v[80:83]}"(va1),
                     "+{v[84:87]}"(vb0), "+{v[88:91]}"(vb1), "+{v[96:99]}"(pz), "+{v100}"(kad), "+{v101}"(vad), [n] "+s"(np)
                   : "{v[32:47]}"(c), "{v[64:67]}"(q), [msk] "s"(vmask)
                   : "v16", "v17", "v18", "v19", "v20", "v21", "v22", "v23", "v24", "v25", "v26", "v27", "v28", "v29", "v30", "v31",
                     "v68", "v69", "v70", "v71", "v92", "v93", "v94", "v95", "scc", "memory");
    }
    if (attempt) break;
    float a = 0.f, s = o[8];
#pragma unroll
    for (int i = 0; i < 8; ++i) { a = fmaxf(a, fabsf(o[i])); s += o[i] * 0.f; }
    const bool ok = (s < 3.0e38f) && (a >= 7.8886091e-31f);
    if (!__any(!ok)) break;
  }
  TS(2, 2);
  __syncthreads();
  char* ex = smem + (w & 7) * 3072 + lane * 16;
  if (kh) {
    *(f32x4*)(ex) = f32x4{o[0], o[1], o[2], o[3]};
    *(f32x4*)(ex + 1024) = f32x4{o[4], o[5], o[6], o[7]};
    *(f32x2*)(ex + 2048) = f32x2{o[8], off};
  }
  __syncthreads();
  if (!kh) {
    const f32x4 g0 = *(const f32x4*)(ex), g1 = *(const f32x4*)(ex + 1024);
    const f32x2 g2 = *(const f32x2*)(ex + 2048);
    const float m = fmaxf(off, g2[1]);
    float fk = __builtin_amdgcn_exp2f(off - m), fg = __builtin_amdgcn_exp2f(g2[1] - m);
    const float inv = 1.f / (o[8] * fk + g2[0] * fg);
    fk *= inv; fg *= inv;
    const f32x4 k0 = f32x4{o[0], o[1], o[2], o[3]} * fk + g0 * fg, k1 = f32x4{o[4], o[5], o[6], o[7]} * fk + g1 * fg;
    const u32x4 ov = {pack2(k0[0], k0[1]), pack2(k0[2], k0[3]), pack2(k1[0], k1[1]), pack2(k1[2], k1[3])};
    st16(ws + WS_OF + ((size_t)(rt * 32 + head) * 64 + lane) * 16, ov);
  }
  TS(2, 6);
}

template <int KS, int DEPTH, bool FINAL, int NW>
DEV void k46_gemm(const u16* __restrict__ res, const char* __restrict__ af, const char* __restrict__ wf,
                                                   char* __restrict__ ws, u16* __restrict__ out) {
  __shared__ __attribute__((aligned(16))) float red[NW * 64 * 68];
  const int tid = threadIdx.x, lane = tid & 63;
  const int w = __builtin_amdgcn_readfirstlane(tid >> 6);
  const int b = blockIdx.x;
  const int rb = (b & 7) * 4 + ((b >> 3) & 3), cbk = b >> 5;
  constexpr int N = KS / NW, E = 8 / NW;
  TS(FINAL ? 5 : 3, 0);
  const unsigned l16 = lane * 16;
  const char* ap = af + (size_t)(rb * 2 * KS + w * N) * 1024;
  const char* wp = wf + (size_t)(cbk * 2 * KS + w * N) * 1024;
  u32x4 fa[DEPTH][2], fw[DEPTH][2];
#define LD4(s, k)                                                                                     \
  {                                                                                                   \
    GLD(fa[s][0], l16, ap + ((k) >> 2) * 4096, ((k) & 3) * 1024);                                     \
    GLD(fw[s][0], l16, wp + ((k) >> 2) * 4096, ((k) & 3) * 1024);                                     \
    GLD(fa[s][1], l16, ap + KS * 1024 + ((k) >> 2) * 4096, ((k) & 3) * 1024);                         \
    GLD(fw[s][1], l16, wp + KS * 1024 + ((k) >> 2) * 4096, ((k) & 3) * 1024);                         \
  }
#pragma unroll
  for (int i = 0; i < DEPTH; ++i) LD4(i, i)
  const int cg = tid & 7, r0 = tid >> 3;
  const int col = cbk * 64 + cg * 8;
  const float* modf = (const float*)(ws + WS_MODF);
  u32x4 xr[2], g0u, g1u, c0u, c1u;
  {
    const char* rp = (const char*)(res + (size_t)(rb * 64) * 512 + cbk * 64);
    const unsigned ro = r0 * 1024 + cg * 16;
    GLD(xr[0], ro, rp, 0);
    if (E == 2) { GLD(xr[1], ro, rp + 32768, 0); } else xr[1] = u32x4{0, 0, 0, 0};
    const unsigned mo = cg * 32;
    const char* gp = (const char*)(modf + (FINAL ? 2560 : 1024) + cbk * 64);
    GLD(g0u, mo, gp, 0); GLD(g1u, mo, gp, 16);
    const char* cp = (const char*)(modf + 2048 + cbk * 64);
    GLD(c0u, mo, cp, 0); GLD(c1u, mo, cp, 16);
  }
  f32x16 acc[2][2];
  acc[0][0] = acc[0][1] = acc[1][0] = acc[1][1] = zero16();
#pragma unroll
  for (int k = 0; k < N; ++k) {
    const int s = k % DEPTH;
    const int after = 4 * ((k + DEPTH < N ? k + DEPTH : N) - k - 1) + (k < DEPTH ? 4 + E : 0);
    asm volatile("s_waitcnt vmcnt(%4)" : "+v"(fa[s][0]), "+v"(fa[s][1]), "+v"(fw[s][0]), "+v"(fw[s][1]) : "n"(after));
    if (k == 0) TS(FINAL ? 5 : 3, 1);
    acc[0][0] = mfma(fw[s][0], fa[s][0], acc[0][0]);
    acc[0][1] = mfma(fw[s][0], fa[s][1], acc[0][1]);
    acc[1][0] = mfma(fw[s][1], fa[s][0], acc[1][0]);
    acc[1][1] = mfma(fw[s][1], fa[s][1], acc[1][1]);
    if (k + DEPTH < N) LD4(s, k + DEPTH)
  }
  asm volatile("s_waitcnt vmcnt(0)" : "+v"(xr[0]), "+v"(xr[1]), "+v"(g0u), "+v"(g1u), "+v"(c0u), "+v"(c1u));
  TS(FINAL ? 5 : 3, 2);
  const f32x4 g0 = __builtin_bit_cast(f32x4, g0u), g1 = __builtin_bit_cast(f32x4, g1u);
  const f32x4 c0 = __builtin_bit_cast(f32x4, c0u) + 1.f, c1 = __builtin_bit_cast(f32x4, c1u) + 1.f;
  {
    float* rp = red + w * (64 * 68) + (lane & 31) * 68 + (lane >> 5) * 4;
#pragma unroll
    for (int i = 0; i < 2; ++i)
#pragma unroll
      for (int j = 0; j < 2; ++j)
#pragma unroll
        for (int q = 0; q < 4; ++q)
          *(f32x4*)(rp + j * 32 * 68 + i * 32 + q * 8) =
              f32x4{acc[i][j][4 * q], acc[i][j][4 * q + 1], acc[i][j][4 * q + 2], acc[i][j][4 * q + 3]};
  }
  __syncthreads();
#pragma unroll
  for (int e = 0; e < E; ++e) {
    const int r = r0 + 32 * e, row = rb * 64 + r;
    f32x4 s0 = {0, 0, 0, 0}, s1 = s0;
#pragma unroll
    for (int p = 0; p < NW; ++p) {
      const float* rp = red + p * (64 * 68) + r * 68 + cg * 8;
      s0 += *(const f32x4*)rp; s1 += *(const f32x4*)(rp + 4);
    }
    const f32x4 x0 = {bflo(xr[e][0]), bfhi(xr[e][0]), bflo(xr[e][1]), bfhi(xr[e][1])};
    const f32x4 x1 = {bflo(xr[e][2]), bfhi(xr[e][2]), bflo(xr[e][3]), bfhi(xr[e][3])};
    s0 = x0 + g0 * s0; s1 = x1 + g1 * s1;
    const u32x4 o = {pack2(s0[0], s0[1]), pack2(s0[2], s0[3]), pack2(s1[0], s1[1]), pack2(s1[2], s1[3])};
    if (FINAL) *(u32x4*)(out + (size_t)row * 512 + col) = o; else st16(out + (size_t)row * 512 + col, o);
    if (!FINAL) {
      const f32x4 y0 = s0 * c0, y1 = s1 * c1;
      const u32x4 y = {pack2(y0[0], y0[1]), pack2(y0[2], y0[3]), pack2(y1[0], y1[1]), pack2(y1[2], y1[3])};
      st16(ws + WS_YF + (size_t)((row >> 5) * 32 + (col >> 4)) * 1024 + ((col >> 3) & 1) * 512 + (row & 31) * 16, y);
      const float mean = red8(((s0[0] + s0[1]) + (s0[2] + s0[3])) + ((s1[0] + s1[1]) + (s1[2] + s1[3]))) * (1.f / 64.f);
      s0 -= mean; s1 -= mean;
      s0 *= s0; s1 *= s1;
      const float m2 = red8(((s0[0] + s0[1]) + (s0[2] + s0[3])) + ((s1[0] + s1[1]) + (s1[2] + s1[3])));
      if (cg == 0) *(f32x2*)(ws + WS_STAT + (size_t)(row * 8 + cbk) * 8) = f32x2{mean, m2};
    }
  }
  TS(FINAL ? 5 : 3, 6);
}

extern "C" __global__ __launch_bounds__(K4W * 64, 2) void k4_oproj(const u16* __restrict__ res, char* __restrict__ ws) {
  k46_gemm<32, K4D, false, K4W>(res, ws + WS_OF, ws + WS_WOF, ws, (u16*)(ws + WS_X1));
}

__global__ __launch_bounds__(256, K5B) void k5_ffn(char* __restrict__ ws) {
  __shared__ __attribute__((aligned(16))) char smem[5 * 28672 + 7168];
  const int tid = threadIdx.x, lane = tid & 63;
  const int w = __builtin_amdgcn_readfirstlane(tid >> 6);
  const int b = blockIdx.x, xc = b & 7, idx = b >> 3;
  const int rb = (xc & 1) * 8 + (idx & 7), cbk = (xc >> 1) * 4 + (idx >> 3);
  const int rt = rb * 4 + w, t0 = cbk * 7;
  TS(4, 0);
  const unsigned sbase = (unsigned)(uintptr_t)smem;
  const unsigned l16 = lane * 16;
  const char* wsrc = ws + WS_WGUF + (size_t)t0 * 33 * 1024;
  const char* ysrc = ws + WS_YF + (size_t)rt * 32 * 1024;
  u32x4 st[4], y[32];
  {
    const char* sp = ws + WS_STAT + (size_t)(rt * 32) * 64;
    const unsigned so = (lane & 31) * 64;
    GLD(st[0], so, sp, 0); GLD(st[1], so, sp, 16); GLD(st[2], so, sp, 32); GLD(st[3], so, sp, 48);
    const int i2 = w + 4 < 7 ? w + 4 : 6;
    dma16(l16, wsrc + (w * 33 + 32) * 1024, sbase + 5 * 28672 + w * 1024);
    dma16(l16, wsrc + (i2 * 33 + 32) * 1024, sbase + 5 * 28672 + i2 * 1024);
  }
#define ISSUE(c)                                                                                             \
  {                                                                                                          \
    _Pragma("unroll") for (int i = 0; i < 7; ++i)                                                            \
        dma16(l16, wsrc + (i * 33 + 4 * (c) + w) * 1024, sbase + ((c) % 5) * 28672 + (w * 7 + i) * 1024);    \
    const char* yp = ysrc + (c) * 4096;                                                                      \
    GLD(y[4 * (c)], l16, yp, 0); GLD(y[4 * (c) + 1], l16, yp, 1024);                                         \
    GLD(y[4 * (c) + 2], l16, yp, 2048); GLD(y[4 * (c) + 3], l16, yp, 3072);                                  \
  }
#define WAITB(n, c)                                                                                          \
  asm volatile("s_waitcnt vmcnt(%4)\n\ts_barrier"                                                            \
               : "+v"(y[4 * (c)]), "+v"(y[4 * (c) + 1]), "+v"(y[4 * (c) + 2]), "+v"(y[4 * (c) + 3]) : "n"(n) : "memory")
#define UP 2
  ISSUE(0) ISSUE(1)
  if (UP > 2) ISSUE(2)
  f32x16 acc[7];
#pragma unroll
  for (int i = 0; i < 7; ++i) { acc[i] = zero16(); asm volatile("" : "+v"(acc[i])); }
  const char* lp = smem + lane * 16;
  u32x4 wc[7], wn[7];
  WAITB(11 * (UP - 1), 0);
  asm volatile("" : "+v"(st[0]), "+v"(st[1]), "+v"(st[2]), "+v"(st[3]));
#pragma unroll
  for (int i = 0; i < 7; ++i) wc[i] = *(const u32x4*)(lp + i * 1024);
  u32x4 we[7];
#pragma unroll
  for (int i = 0; i < 7; ++i) we[i] = *(const u32x4*)(lp + 5 * 28672 + i * 1024);
  TS(4, 1);
  float r2;
  u32x4 yx;
  {
    float mb[8], m2 = 0.f, mean = 0.f;
#pragma unroll
    for (int i = 0; i < 4; ++i) {
      mb[2 * i] = __uint_as_float(st[i][0]); mb[2 * i + 1] = __uint_as_float(st[i][2]);
      m2 += __uint_as_float(st[i][1]) + __uint_as_float(st[i][3]);
      mean += mb[2 * i] + mb[2 * i + 1];
    }
    mean *= 0.125f;
    float d = 0.f;
#pragma unroll
    for (int i = 0; i < 8; ++i) d += (mb[i] - mean) * (mb[i] - mean);
    const float var = (m2 + 64.f * d) * (1.f / 512.f) + 1e-5f;
    r2 = 1.f / var;
    unsigned mh, ml, rh, rlo;
    split_bf(-mean, mh, ml); split_bf(sqrtf(var), rh, rlo);
    yx = u32x4{mh | (mh << 16), ml | (ml << 16), rh | (rh << 16), rlo | (rlo << 16)};
    if (lane >= 32) yx = u32x4{0, 0, 0, 0};
  }
#define MFV(c, a, b) asm volatile("v_mfma_f32_32x32x16_bf16 %0, %1, %2, %0" : "+v"(c) : "v"(a), "v"(b))
#define KSTEP(g, CUR, NXT, ic, ib)                                                                                   \
  {                                                                                                          \
    const char* np = lp + ((((g) + 1) / 4) % 5) * 28672 + (((g) + 1) % 4) * 7168;                            \
    _Pragma("unroll") for (int i = 0; i < 7; ++i) {                                                          \
      if ((g) < 31) NXT[i] = *(const u32x4*)(np + i * 1024);                                                          \
      __builtin_amdgcn_sched_barrier(6);                                                                     \
      MFV(acc[i], CUR[i], y[g]);                                                                             \
      __builtin_amdgcn_sched_barrier(6);                                                                     \
      const int n = 7 * (g) + i;                                                                             \
      const bool early = n < (5 - UP) * 11;                                                                  \
      const int lc = early ? UP + n / 11 : (ic), j = early ? n % 11 : (ib) + i;                              \
      if (early || (lc >= 5 && lc < 8)) {                                                                    \
        if (j < 7) dma16(l16, wsrc + (j * 33 + 4 * lc + w) * 1024, sbase + (lc % 5) * 28672 + (w * 7 + j) * 1024); \
        else if (j < 11) GLD(y[(4 * lc + j - 7) & 31], l16, ysrc + lc * 4096, ((j - 7) & 3) * 1024);         \
      }                                                                                                      \
      __builtin_amdgcn_sched_barrier(6);                                                                     \
    }                                                                                                        \
  }
#define CHUNK(c, PRE)                                                                                        \
  KSTEP(4 * (c), wc, wn, (c) + 3, 7) KSTEP(4 * (c) + 1, wn, wc, 0, 0) KSTEP(4 * (c) + 2, wc, wn, 0, 0) PRE KSTEP(4 * (c) + 3, wn, wc, (c) + 4, 0)
#define CORR                                                                                                 \
  { asm volatile("s_nop 3" : "+v"(yx));                                                                                                      \
    _Pragma("unroll") for (int i = 0; i < 7; ++i) MFV(acc[i], we[i], yx);                                     \
    __builtin_amdgcn_sched_barrier(6);                                                                       \
  }
  CHUNK(0, CORR WAITB(11 * UP - 1, 1);)
  CHUNK(1, WAITB(22, 2);)
  TS(4, 3);
  CHUNK(2, WAITB(22, 3);)
  CHUNK(3, WAITB(22, 4);)
  TS(4, 4);
  CHUNK(4, WAITB(22, 5);)
  CHUNK(5, WAITB(11, 6);)
  TS(4, 5);
  CHUNK(6, WAITB(0, 7);)
  CHUNK(7, )
  asm volatile("s_nop 15\n\ts_nop 3" : "+v"(acc[0]), "+v"(acc[1]), "+v"(acc[2]), "+v"(acc[3]), "+v"(acc[4]), "+v"(acc[5]), "+v"(acc[6]));
  TS(4, 2);
  char* hp = ws + WS_HHF + ((size_t)(rt * 112 + t0) * 64 + lane) * 16;
#pragma unroll
  for (int i = 0; i < 7; ++i) {
    const f32x16 a = acc[i];
    float hv[8];
#pragma unroll
    for (int j = 0; j < 8; ++j) {
      float m;
      asm("v_max_f32 %0, 0, %1" : "=v"(m) : "v"(a[j]));
      hv[j] = m * r2 * a[j + 8];
    }
    const u32x4 ho = {pack2(hv[0], hv[1]), pack2(hv[2], hv[3]), pack2(hv[4], hv[5]), pack2(hv[6], hv[7])};
    st16(hp + i * 1024, ho);
  }
  TS(4, 6);
}

extern "C" __global__ __launch_bounds__(K6W * 64, 2) void k6_down(char* __restrict__ ws, u16* __restrict__ out) {
  k46_gemm<112, K6D, true, K6W>((const u16*)(ws + WS_X1), ws + WS_HHF, ws + WS_WDF, ws, out);
}

extern "C" void kernel_launch(void* const* d_in, const int* in_sizes, int n_in, void* d_out, int out_size, void* d_ws,
                              size_t ws_size, hipStream_t stream) {
  const u16* x = (const u16*)d_in[0];
  const u16* wq = (const u16*)d_in[1];
  const u16* wk = (const u16*)d_in[2];
  const u16* wv = (const u16*)d_in[3];
  const u16* wo = (const u16*)d_in[4];
  const u16* wg = (const u16*)d_in[6];
  const u16* wu = (const u16*)d_in[7];
  const u16* wd = (const u16*)d_in[8];
  const u16* cc = (const u16*)d_in[9];
  const u16* wmod = (const u16*)d_in[10];
  char* ws = (char*)d_ws;
    k1_prep<<<232, 256, 0, stream>>>(wq, wk, wv, cc, wmod, ws);
  k2_qkv<<<376, 512, 0, stream>>>(x, wg, wu, ws);
  k3_attn<<<256, 1024, 0, stream>>>(wo, wg, wu, wd, ws);
  k4_oproj<<<256, K4W * 64, 0, stream>>>(x, ws);
  k5_ffn<<<256, 256, 0, stream>>>(ws);
  k6_down<<<256, K6W * 64, 0, stream>>>(ws, (u16*)d_out);
}
```
